```python
import torch

def _softmax(x, axis=-1):
    return torch.softmax(x, dim=axis)

def _gelu(x):
    return torch.nn.functional.gelu(x, approximate='tanh')

def _silu(x):
    return torch.nn.functional.silu(x)

def _sigmoid(x):
    return torch.sigmoid(x)

def _layernorm(x, axis=-1, eps=1e-05):
    m = x.mean(dim=axis, keepdim=True)
    v = x.var(dim=axis, keepdim=True, unbiased=False)
    return (x - m) / torch.sqrt(v + eps)

def _scatter_add(tgt, idx, src, axis=0):
    return tgt.clone().index_add_(axis, idx, src)

def _segment_sum(src, seg, n):
    out = torch.zeros((n,) + src.shape[1:], dtype=src.dtype)
    return out.index_add_(0, seg, src)

def _pad(x, pw):
    flat = tuple((v for pair in reversed(pw) for v in pair))
    return torch.nn.functional.pad(x, flat)
kernel_name = 'adaLN_cond__spectral_gfnet__glu_gated_bilinear__prompt'

def setup_inputs():

    _w = lambda a: (lambda t: t.to(torch.bfloat16) if t.is_floating_point() else t)(torch.as_tensor(a))

    def _A_setup():
        B, S, D = (1, 2048, 640)
        Df = D // 2 + 1
        return {'x': _w((lambda *s: torch.randn(*s, dtype=torch.bfloat16) * 0.05)(B, S, D)), 'w_r': _w((lambda *s: torch.randn(*s, dtype=torch.bfloat16) * 1.6)(S, Df) * 0.02), 'w_i': _w((lambda *s: torch.randn(*s, dtype=torch.bfloat16) * 1.6)(S, Df) * 0.02)}
    _A_in = _A_setup()

    def _B_setup():
        B, S, D, F = (1, 2048, 640, 1728)
        return {'x': _w((lambda *s: torch.randn(*s, dtype=torch.bfloat16) * 0.05)(B, S, D)), 'w_gate': _w((lambda *s: torch.randn(*s, dtype=torch.bfloat16) * 0.4)(D, F) * D ** (-0.5)), 'w_up': _w((lambda *s: torch.randn(*s, dtype=torch.bfloat16) * 0.4)(D, F) * D ** (-0.5)), 'w_down': _w((lambda *s: torch.randn(*s, dtype=torch.bfloat16) * 0.4)(F, D) * F ** (-0.5))}
    _B_in = _B_setup()
    return {'A_x': _A_in['x'], 'A_w_r': _A_in['w_r'], 'A_w_i': _A_in['w_i'], 'B_x': _B_in['x'], 'B_w_gate': _B_in['w_gate'], 'B_w_up': _B_in['w_up'], 'B_w_down': _B_in['w_down'], 'c': _w((lambda *s: torch.randn(*s, dtype=torch.bfloat16) * 0.1)(1, 640) * 8.0), 'w_mod': _w((lambda *s: torch.randn(*s, dtype=torch.bfloat16) * 0.1)(640, 6 * 640) * 640 ** (-0.5) * 8.0)}

def reference(A_x, A_w_r, A_w_i, B_x, B_w_gate, B_w_up, B_w_down, c, w_mod):

    def _A_ref(x, w_r, w_i):
        B, S, D = x.shape
        X = (lambda x, axes=(-2, -1): torch.fft.rfft2(x.float(), dim=axes))(x, axes=(-2, -1))
        W = w_r + 1j * w_i
        X = X * W
        y = (lambda x, s, axes=(-2, -1): torch.fft.irfft2(x, s=s, dim=axes).to(torch.bfloat16))(X, (S, D), axes=(-2, -1))
        return y

    def _B_ref(x, w_gate, w_up, w_down):
        g = torch.einsum('bsd,df->bsf', x, w_gate)
        u = torch.einsum('bsd,df->bsf', x, w_up)
        h = g * u
        return torch.einsum('bsf,fd->bsd', h, w_down)
    x = A_x
    mod = (_silu(c) @ w_mod).reshape(c.shape[0], 6, 1, -1)
    sa, ca, ga, sb, cb, gb = (mod[:, 0].clone(), mod[:, 1].clone(), mod[:, 2].clone(), mod[:, 3].clone(), mod[:, 4].clone(), mod[:, 5].clone())
    h = _layernorm(x, axis=-1) * (1 + ca) + sa
    x = x + ga * _A_ref(h, A_w_r, A_w_i)
    h = _layernorm(x, axis=-1) * (1 + cb) + sb
    x = x + gb * _B_ref(h, B_w_gate, B_w_up, B_w_down)
    return x
```

```cpp
#include <hip/hip_runtime.h>
#include <stdint.h>

typedef unsigned int u32;
typedef unsigned short u16;
typedef _Float16 h2 __attribute__((ext_vector_type(2)));
typedef _Float16 h8 __attribute__((ext_vector_type(8)));
typedef __bf16 b2 __attribute__((ext_vector_type(2)));
typedef __bf16 b8 __attribute__((ext_vector_type(8)));
typedef float f2 __attribute__((ext_vector_type(2)));
typedef float f4 __attribute__((ext_vector_type(4)));
typedef u32 u2 __attribute__((ext_vector_type(2)));
typedef u32 u4 __attribute__((ext_vector_type(4)));
typedef short s4 __attribute__((ext_vector_type(4)));
typedef __attribute__((address_space(3))) s4* lds_s4p;
#define DI __device__ __forceinline__

constexpr int S_ = 2048, D_ = 640, F_ = 1728, DF_ = 321;
constexpr int ROWP = 2568;
constexpr int NCH = 31;

constexpr size_t WS_MOD = 0;
constexpr size_t WS_SCL = 16384;
constexpr size_t WS_CA2 = 20480;
constexpr size_t WS_CB2 = WS_CA2 + 15 * 1024;
constexpr size_t WS_T1 = 65536;
constexpr size_t SZ_T = (size_t)321 * 256 * 32;
constexpr size_t WS_T2 = WS_T1 + SZ_T;
constexpr size_t WS_WT = WS_T2 + SZ_T;
constexpr size_t WS_H2 = WS_WT + (size_t)321 * 8192;
constexpr size_t WS_X1 = WS_H2 + (size_t)S_ * D_ * 2;
constexpr size_t WS_WGU = WS_X1 + (size_t)S_ * D_ * 2;
constexpr size_t WS_TT = WS_WGU + (size_t)NCH * 20 * 7 * 1024;
constexpr size_t WS_WD = WS_TT + (size_t)128 * 216 * 256;
constexpr size_t WS_END = WS_WD + (size_t)8 * 54 * 5 * 1024;

DI float bf2f(u32 v) { return __uint_as_float(v << 16); }
DI float bflo(u32 v) { return __uint_as_float(v << 16); }
DI float bfhi(u32 v) { return __uint_as_float(v & 0xffff0000u); }
DI u32 pkbf(float a, float b) { f2 t = {a, b}; return __builtin_bit_cast(u32, __builtin_convertvector(t, b2)); }
DI float rbf(float a) { return bflo(pkbf(a, a)); }
DI u32 pkh(float a, float b) { f2 t = {a, b}; return __builtin_bit_cast(u32, __builtin_convertvector(t, h2)); }
DI float vcos(float rev) { return __builtin_amdgcn_cosf(rev); }
DI float vsin(float rev) { return __builtin_amdgcn_sinf(rev); }
DI f4 mfma_h(u4 a, u4 b, f4 c) {
  return __builtin_amdgcn_mfma_f32_16x16x32_f16(__builtin_bit_cast(h8, a), __builtin_bit_cast(h8, b), c, 0, 0, 0);
}
DI f4 mfma_b(u4 a, u4 b, f4 c) {
  return __builtin_amdgcn_mfma_f32_16x16x32_bf16(__builtin_bit_cast(b8, a), __builtin_bit_cast(b8, b), c, 0, 0, 0);
}
DI u2 trread(const char* p) {
  s4 v = __builtin_amdgcn_ds_read_tr16_b64_v4i16((lds_s4p)p);
  return __builtin_bit_cast(u2, v);
}
DI u4 cat(u2 a, u2 b) { return u4{a[0], a[1], b[0], b[1]}; }
DI u4 neg_h8(u4 a) { return a ^ 0x80008000u; }
DI int tok_row(int tile, int c) { return 4 * tile + 2 * (c >> 3) + 512 * ((c & 7) >> 1) + (c & 1); }

DI float row16_sum(float v) {
  v += __builtin_bit_cast(float, __builtin_amdgcn_update_dpp(0, __builtin_bit_cast(int, v), 0x128, 0xf, 0xf, false));
  v += __builtin_bit_cast(float, __builtin_amdgcn_update_dpp(0, __builtin_bit_cast(int, v), 0x124, 0xf, 0xf, false));
  v += __builtin_bit_cast(float, __builtin_amdgcn_update_dpp(0, __builtin_bit_cast(int, v), 0x122, 0xf, 0xf, false));
  v += __builtin_bit_cast(float, __builtin_amdgcn_update_dpp(0, __builtin_bit_cast(int, v), 0x121, 0xf, 0xf, false));
  return v;
}
DI float row16_max(float v) {
  v = fmaxf(v, __builtin_bit_cast(float, __builtin_amdgcn_update_dpp(0, __builtin_bit_cast(int, v), 0x128, 0xf, 0xf, false)));
  v = fmaxf(v, __builtin_bit_cast(float, __builtin_amdgcn_update_dpp(0, __builtin_bit_cast(int, v), 0x124, 0xf, 0xf, false)));
  v = fmaxf(v, __builtin_bit_cast(float, __builtin_amdgcn_update_dpp(0, __builtin_bit_cast(int, v), 0x122, 0xf, 0xf, false)));
  v = fmaxf(v, __builtin_bit_cast(float, __builtin_amdgcn_update_dpp(0, __builtin_bit_cast(int, v), 0x121, 0xf, 0xf, false)));
  return v;
}
DI float half32_sum(float v) {
  v = row16_sum(v);
  u32 u = __float_as_uint(v);
  auto r = __builtin_amdgcn_permlane16_swap(u, u, false, false);
  return __uint_as_float(r[0]) + __uint_as_float(r[1]);
}
DI float half32_max(float v) {
  v = row16_max(v);
  u32 u = __float_as_uint(v);
  auto r = __builtin_amdgcn_permlane16_swap(u, u, false, false);
  return fmaxf(__uint_as_float(r[0]), __uint_as_float(r[1]));
}
DI float wave_sum(float v) {
  v = half32_sum(v);
  u32 u = __float_as_uint(v);
  auto r = __builtin_amdgcn_permlane32_swap(u, u, false, false);
  return __uint_as_float(r[0]) + __uint_as_float(r[1]);
}

constexpr int K0_GEMV = 240, K0_WT = 80, K0_WSP = 4, K0_CONST = 9, K0_WGU = 155, K0_WD = 54;
constexpr int K0_B1 = K0_GEMV, K0_B2 = K0_B1 + K0_WT, K0_B3 = K0_B2 + K0_WSP, K0_B4 = K0_B3 + K0_CONST,
              K0_B5 = K0_B4 + K0_WGU, K0_B6 = K0_B5 + K0_WD;

DI void job_gemv(int bid, const u16* __restrict__ c, const u16* __restrict__ wmod, float* __restrict__ mod, char* lds) {
  const int cb = (bid & 7) * 30 + (bid >> 3);
  const int n0 = cb * 16;
  const int t = threadIdx.x, half = t & 1, rl = t >> 1;
  u4 w[5];
  float s[5];
#pragma unroll
  for (int i = 0; i < 5; i++) {
    const int k = rl + 128 * i;
    w[i] = *(const u4*)(wmod + (size_t)k * 3840 + n0 + 8 * half);
    float cv = bf2f(c[k]);
    s[i] = rbf(cv / (1.0f + __expf(-cv)));
  }
  float acc[8];
#pragma unroll
  for (int e = 0; e < 8; e++) acc[e] = 0.f;
#pragma unroll
  for (int i = 0; i < 5; i++) {
#pragma unroll
    for (int e = 0; e < 4; e++) {
      acc[2 * e] += s[i] * bflo(w[i][e]);
      acc[2 * e + 1] += s[i] * bfhi(w[i][e]);
    }
  }
#pragma unroll
  for (int e = 0; e < 8; e++) {
#pragma unroll
    for (int m = 2; m < 64; m <<= 1) acc[e] += __shfl_xor(acc[e], m);
  }
  float* red = (float*)lds;
  const int wv = t >> 6, ln = t & 63;
  if (ln < 2) {
#pragma unroll
    for (int e = 0; e < 8; e++) red[wv * 16 + ln * 8 + e] = acc[e];
  }
  __syncthreads();
  if (t < 16) {
    float v = rbf(red[t] + red[16 + t] + red[32 + t] + red[48 + t]);
    const int n = n0 + t, which = n / 640;
    if (which == 1 || which == 4) v = rbf(1.0f + v);
    mod[n] = v;
  }
}

DI void job_wtile(int id, const u16* __restrict__ wr, const u16* __restrict__ wi, char* __restrict__ wt, char* lds) {
  const int cb = id % 10, m = id / 10;
  u16* img = (u16*)lds;
  constexpr int RS = 34;
  const int t = threadIdx.x;
  for (int it = 0; it < 32; it++) {
    const int idx = it * 256 + t;
    const int seg = idx >> 4, dw = idx & 15;
    const int arr = seg >> 8, i = seg & 255;
    const int R = (i < 128) ? 128 * m + i : 1024 + 128 * m + (i - 128);
    const u16* src = (arr ? wi : wr) + (size_t)R * DF_ + 32 * cb + 2 * dw;
    const u32 v = (u32)src[0] | ((u32)src[1] << 16);
    *(u32*)(img + (arr * 256 + i) * RS + 2 * dw) = v;
  }
  __syncthreads();
  for (int it = 0; it < 8; it++) {
    const int idx = it * 256 + t;
    const int c = idx & 15, k1 = (idx >> 4) & 3, kdl = idx >> 6;
    const int kd = 32 * cb + kdl;
    if (kd == 0) continue;
    const int k0 = k1 + 4 * c;
    u4 o;
#pragma unroll
    for (int e = 0; e < 2; e++) {
      const int i = k0 + 64 * e;
      const u32 w1r = img[(i)*RS + kdl], w1i = img[(256 + i) * RS + kdl];
      const u32 w2r = img[(128 + i) * RS + kdl], w2i = img[(256 + 128 + i) * RS + kdl];
      o[2 * e] = w1r | (w1i << 16);
      o[2 * e + 1] = w2r | (w2i << 16);
    }
    *(u4*)(wt + ((size_t)(kd * 4 + k1) * 2 + (m & 1)) * 1024 + (c + 16 * (m >> 1)) * 16) = o;
  }
}
DI void job_wspecial(int id, const u16* __restrict__ wr, const u16* __restrict__ wi, char* __restrict__ wt) {
  const int idx = id * 256 + threadIdx.x;
  const int lane = idx & 63, half = (idx >> 6) & 1, k1 = (idx >> 7) & 3, kd = (idx >> 9) ? 320 : 0;
  const int c = lane & 15, q = lane >> 4;
  u4 o;
#pragma unroll
  for (int e = 0; e < 2; e++) {
    const int kp = k1 + 4 * c + 64 * (4 * q + 2 * half + e);
#pragma unroll
    for (int z = 0; z < 2; z++) {
      const int ks = kp + 1024 * z, kn = (2048 - ks) & 2047;
      const float re = 0.5f * (bf2f(wr[(size_t)ks * DF_ + kd]) + bf2f(wr[(size_t)kn * DF_ + kd]));
      const float im = 0.5f * (bf2f(wi[(size_t)ks * DF_ + kd]) - bf2f(wi[(size_t)kn * DF_ + kd]));
      o[2 * e + z] = pkbf(re, im);
    }
  }
  *(u4*)(wt + ((size_t)(kd * 4 + k1) * 2 + half) * 1024 + lane * 16) = o;
}
DI int offt(int mt) { return mt == 0 ? 0 : (mt == 1 ? 16 : 24); }
DI void job_const(int id, char* __restrict__ ws) {
  const int fr = id * 4 + (threadIdx.x >> 6);
  if (fr >= 33) return;
  const int l = threadIdx.x & 63, l15 = l & 15, g4 = l >> 4;
  const float rs40 = 0.15811388300841897f;
  float v[8];
  if (fr < 15) {
    const int T = fr / 3, mt = fr % 3;
    const int j2 = 8 * T + 2 * (l15 >> 2) + ((l15 & 3) >> 1), rio = l15 & 1;
#pragma unroll
    for (int j = 0; j < 8; j++) {
      const int n2 = offt(mt) + 4 * g4 + (j & 3), rii = j >> 2;
      const float x = (float)((n2 * j2) % 40) * (1.0f / 40.0f);
      const float cc = vcos(x), ss = -vsin(x);
      float r = (rio == 0) ? (rii == 0 ? cc : -ss) : (rii == 0 ? ss : cc);
      if (mt == 2 && n2 < 32) r = 0.f;
      v[j] = r * rs40;
    }
  } else {
    const int f = fr - 15;
    const int reim = f & 1, kp = (f >> 1) % 3, ct = (f >> 1) / 3;
    const int n2 = offt(ct) + l15;
#pragma unroll
    for (int j = 0; j < 8; j++) {
      const int j2 = 16 * kp + 4 * g4 + (j >> 1), rii = j & 1;
      const float x = (float)((n2 * j2) % 40) * (1.0f / 40.0f);
      const float cc = vcos(x), ss = vsin(x);
      float r = (reim == 0) ? (rii == 0 ? cc : -ss) : (rii == 0 ? ss : cc);
      if (j2 >= 40) r = 0.f;
      v[j] = r * rs40;
    }
  }
  u4 o = {pkh(v[0], v[1]), pkh(v[2], v[3]), pkh(v[4], v[5]), pkh(v[6], v[7])};
  *(u4*)(ws + WS_CA2 + (size_t)fr * 1024 + l * 16) = o;
}
DI void job_wgu(int id, const u16* __restrict__ wg, const u16* __restrict__ wu, char* __restrict__ dst, char* lds) {
  const int j = id % NCH, kg = id / NCH;
  const int t = threadIdx.x;
  constexpr int RS = 7 * 32 + 16;
  for (int it = 0; it < 7; it++) {
    const int idx = it * 256 + t;
    const int i = idx % 7, arr = (idx / 7) & 1, k = idx / 14;
    const int f0 = 56 * j + 8 * i;
    u4 v = {0, 0, 0, 0};
    if (f0 < F_) v = *(const u4*)((arr ? wu : wg) + (size_t)(128 * kg + k) * F_ + f0);
    char* p = lds + k * RS + i * 32 + arr * 4;
#pragma unroll
    for (int m = 0; m < 4; m++) *(u32*)(p + 8 * m) = v[m];
  }
  __syncthreads();
  const int wv = t >> 6, l = t & 63, i16 = l & 15, g4 = l >> 4;
  for (int fr = wv; fr < 28; fr += 4) {
    const int kl = fr / 7, i = fr % 7;
    const char* p = lds + (32 * kl + 8 * g4 + (i16 >> 2)) * RS + i * 32 + 8 * (i16 & 3);
    const u2 a = trread(p), b = trread(p + 4 * RS);
    *(u4*)(dst + ((size_t)((j * 20 + 4 * kg + kl) * 7 + i)) * 1024 + l * 16) = cat(a, b);
  }
}
DI void job_wd(int s, const u16* __restrict__ wd, char* __restrict__ dst, char* lds) {
  const int t = threadIdx.x;
  constexpr int RS = 1280 + 32;
  for (int it = 0; it < 10; it++) {
    const int idx = it * 256 + t;
    const int k = idx / 80, p = idx % 80;
    *(u4*)(lds + k * RS + p * 16) = *(const u4*)(wd + (size_t)(32 * s + k) * D_ + 8 * p);
  }
  __syncthreads();
  const int wv = t >> 6, l = t & 63, i16 = l & 15, g4 = l >> 4;
  for (int fr = wv; fr < 40; fr += 4) {
    const int cc = fr / 5, i = fr % 5;
    const char* p = lds + (8 * g4 + (i16 >> 2)) * RS + (80 * cc + 16 * i) * 2 + 8 * (i16 & 3);
    const u2 a = trread(p), b = trread(p + 4 * RS);
    *(u4*)(dst + ((size_t)((cc * 54 + s) * 5 + i)) * 1024 + l * 16) = cat(a, b);
  }
}

__global__ __launch_bounds__(256) void k0_prep(const u16* __restrict__ c, const u16* __restrict__ wmod,
                                               const u16* __restrict__ wr, const u16* __restrict__ wi,
                                               const u16* __restrict__ wg, const u16* __restrict__ wu,
                                               const u16* __restrict__ wd, char* __restrict__ ws) {
  __shared__ __attribute__((aligned(16))) char lds[43008];
  const int b = blockIdx.x;
  if (b < K0_B1) job_gemv(b, c, wmod, (float*)(ws + WS_MOD), lds);
  else if (b < K0_B2) job_wtile(b - K0_B1, wr, wi, ws + WS_WT, lds);
  else if (b < K0_B3) job_wspecial(b - K0_B2, wr, wi, ws + WS_WT);
  else if (b < K0_B4) job_const(b - K0_B3, ws);
  else if (b < K0_B5) job_wgu(b - K0_B4, wg, wu, ws + WS_WGU, lds);
  else job_wd(b - K0_B5, wd, ws + WS_WD, lds);
}

DI int xcd_map(int b, int per) { return (b & 7) * per + (b >> 3); }
DI int himg(int rho) { return 96 * rho + 128 * (rho >> 3); }
DI int ostage(int pos) { return pos; }

__global__ __launch_bounds__(256) void k1_fwd(const u16* __restrict__ x, char* __restrict__ ws) {
  __shared__ __attribute__((aligned(16))) char lds[14336 + 15360 + ROWP * 4];
  char* H = lds;
  char* CA = lds + 14336;
  u32* O = (u32*)(lds + 14336 + 15360);
  const float* mod = (const float*)(ws + WS_MOD);
  const int s2 = xcd_map(blockIdx.x, 32);
  const int t = threadIdx.x, wv = t >> 6, l = t & 63, l15 = l & 15, g4 = l >> 4;
  const int rr = t >> 5, l32 = t & 31;
  const int row = 2 * s2 + 512 * (rr >> 1) + (rr & 1);
  u2 xv[5];
  f4 av[5], sv[5];
#pragma unroll
  for (int i = 0; i < 5; i++) {
    xv[i] = *(const u2*)(x + (size_t)row * D_ + 128 * i + 4 * l32);
    sv[i] = *(const f4*)(mod + 128 * i + 4 * l32);
    av[i] = *(const f4*)(mod + 640 + 128 * i + 4 * l32);
  }
  for (int p = t; p < 960; p += 256) *(u4*)(CA + p * 16) = *(const u4*)(ws + WS_CA2 + p * 16);
  u4 R0, R1;
  {
    const int par = g4 >> 1;
    float v0[8], v1[8];
#pragma unroll
    for (int j = 0; j < 8; j++) {
      const int n1 = 8 * (g4 & 1) + j;
      const int m = n1 * l15;
      v0[j] = vcos((float)((m - 4 * par) & 15) * (1.0f / 16.0f)) * 0.125f;
      v1[j] = vcos((float)((m + 4 - 4 * par) & 15) * (1.0f / 16.0f)) * 0.125f;
    }
    R0 = u4{pkh(v0[0], v0[1]), pkh(v0[2], v0[3]), pkh(v0[4], v0[5]), pkh(v0[6], v0[7])};
    R1 = u4{pkh(v1[0], v1[1]), pkh(v1[2], v1[3]), pkh(v1[4], v1[5]), pkh(v1[6], v1[7])};
  }
  float twc[3][4], tws[3][4];
#pragma unroll
  for (int mt = 0; mt < 3; mt++)
#pragma unroll
    for (int r = 0; r < 4; r++) {
      const int n2 = offt(mt) + 4 * g4 + r;
      const float a = (float)(n2 * l15) * (1.0f / 640.0f);
      twc[mt][r] = vcos(a);
      tws[mt][r] = -vsin(a);
    }
  float ma = 0.f, ms = 0.f;
#pragma unroll
  for (int i = 0; i < 5; i++)
#pragma unroll
    for (int e = 0; e < 4; e++) {
      ma = fmaxf(ma, fabsf(av[i][e]));
      ms = fmaxf(ms, fabsf(sv[i][e]));
    }
  const float Rh = half32_max(ma) + half32_max(ms);
  int ex = (int)((__float_as_uint(Rh) >> 23) & 255) - 127 + 1;
  ex = ex < -100 ? -100 : (ex > 100 ? 100 : ex);
  const float sh = __uint_as_float((u32)(127 - ex) << 23);
  if (blockIdx.x == 0 && t == 0) *(float*)(ws + WS_SCL) = __uint_as_float((u32)(127 + ex) << 23);
  float xf[20];
  float sum = 0.f;
#pragma unroll
  for (int i = 0; i < 5; i++) {
    xf[4 * i] = bflo(xv[i][0]); xf[4 * i + 1] = bfhi(xv[i][0]);
    xf[4 * i + 2] = bflo(xv[i][1]); xf[4 * i + 3] = bfhi(xv[i][1]);
    sum += (xf[4 * i] + xf[4 * i + 1]) + (xf[4 * i + 2] + xf[4 * i + 3]);
  }
  const float mean = half32_sum(sum) * (1.0f / 640.0f);
  float sq = 0.f;
#pragma unroll
  for (int e = 0; e < 20; e++) { xf[e] -= mean; sq += xf[e] * xf[e]; }
  const float var = half32_sum(sq) * (1.0f / 640.0f);
  const float rstd = rsqrtf(var + 1e-5f);
#pragma unroll
  for (int i = 0; i < 5; i++) {
    float hv[4];
#pragma unroll
    for (int e = 0; e < 4; e++) hv[e] = (xf[4 * i + e] * rstd * av[i][e] + sv[i][e]) * sh;
    const int d = 128 * i + 4 * l32, n1 = d / 40, n2 = d % 40;
    *(u2*)(H + himg(16 * rr + n1) + 2 * n2) = u2{pkh(hv[0], hv[1]), pkh(hv[2], hv[3])};
  }
  __syncthreads();
  const int k1 = wv;
  const u4 R2 = neg_h8(R0), R3 = neg_h8(R1);
  f4 dre[3], dim[3];
#pragma unroll
  for (int mt = 0; mt < 3; mt++) { dre[mt] = f4{0, 0, 0, 0}; dim[mt] = f4{0, 0, 0, 0}; }
#pragma unroll
  for (int kap = 0; kap < 4; kap++) {
    const int mu = (kap * k1) & 3;
    const u4 Bre = mu == 0 ? R0 : (mu == 1 ? R1 : (mu == 2 ? R2 : R3));
    const u4 Bim = mu == 0 ? R1 : (mu == 1 ? R2 : (mu == 2 ? R3 : R0));
#pragma unroll
    for (int mt = 0; mt < 3; mt++) {
      const int rho = 32 * kap + 8 * g4 + (l15 >> 2);
      const char* p0 = H + himg(rho) + 2 * offt(mt) + 8 * (l15 & 3);
      const char* p1 = H + himg(rho + 4) + 2 * offt(mt) + 8 * (l15 & 3);
      const u4 A = cat(trread(p0), trread(p1));
      dre[mt] = mfma_h(A, Bre, dre[mt]);
      dim[mt] = mfma_h(A, Bim, dim[mt]);
    }
  }
  u4 Bf[3];
#pragma unroll
  for (int mt = 0; mt < 3; mt++) {
    float re[4], im[4];
#pragma unroll
    for (int r = 0; r < 4; r++) {
      re[r] = dre[mt][r] * twc[mt][r] - dim[mt][r] * tws[mt][r];
      im[r] = dre[mt][r] * tws[mt][r] + dim[mt][r] * twc[mt][r];
    }
    Bf[mt] = u4{pkh(re[0], re[1]), pkh(re[2], re[3]), pkh(im[0], im[1]), pkh(im[2], im[3])};
  }
#pragma unroll
  for (int T = 0; T < 5; T++) {
    f4 acc = {0, 0, 0, 0};
#pragma unroll
    for (int mt = 0; mt < 3; mt++) acc = mfma_h(*(const u4*)(CA + (T * 3 + mt) * 1024 + l * 16), Bf[mt], acc);
#pragma unroll
    for (int i = 0; i < 2; i++) {
      const int kd = l15 + 16 * (8 * T + 2 * g4 + i);
      const u32 v = pkh(acc[2 * i], acc[2 * i + 1]);
      const int alt = 2 * ((4 - k1) & 3) + 1;
      const int pos = kd <= 320 ? 8 * kd + 2 * k1 : 8 * (640 - kd) + alt;
      O[ostage(pos)] = v;
      if (kd == 0 || kd == 320) O[ostage(8 * kd + alt)] = v;
    }
  }
  __syncthreads();
  char* T1 = ws + WS_T1;
  for (int p = t; p < 642; p += 256) {
    const u4 v = *(const u4*)(O + 4 * p);
    *(u4*)(T1 + ((size_t)(p >> 1) * 256 + s2) * 32 + (p & 1) * 16) = v;
  }
}

__global__ __launch_bounds__(256) void k2_filter(char* __restrict__ ws) {
  __shared__ __attribute__((aligned(16))) u32 O[256 * 8];
  const int b = blockIdx.x;
  int g;
  {
    const int nfull = 320;
    if (b < nfull) g = ((b & 7) * 10 + ((b >> 3) >> 2)) * 4 + ((b >> 3) & 3);
    else g = b;
  }
  const int t = threadIdx.x, k1 = t >> 6, l = t & 63, l15 = l & 15, g4 = l >> 4;
  const char* T1 = ws + WS_T1 + (size_t)g * 8192;
  u2 pv[4];
#pragma unroll
  for (int i = 0; i < 4; i++) pv[i] = *(const u2*)(T1 + (16 * (4 * g4 + i) + l15) * 32 + 8 * k1);
  const char* Wt = ws + WS_WT + (size_t)(g * 4 + k1) * 2048;
  const u4 w0 = *(const u4*)(Wt + l * 16), w1 = *(const u4*)(Wt + 1024 + l * 16);
  u4 B1re, B1im;
  {
    float cs[4], sn[4];
#pragma unroll
    for (int i = 0; i < 4; i++) {
      const float a = (float)(((4 * g4 + i) * (4 * l15 + k1)) & 63) * (1.0f / 64.0f);
      cs[i] = vcos(a) * 0.25f;
      sn[i] = vsin(a) * 0.25f;
    }
#pragma unroll
    for (int i = 0; i < 4; i++) { B1re[i] = pkh(cs[i], sn[i]); B1im[i] = pkh(-sn[i], cs[i]); }
  }
  u2 C2, S2;
  {
    float cs[4], sn[4];
#pragma unroll
    for (int j = 0; j < 4; j++) {
      const float a = (float)(((4 * g4 + j) * l15) & 15) * (1.0f / 16.0f);
      cs[j] = vcos(a) * 0.25f;
      sn[j] = vsin(a) * 0.25f;
    }
    C2 = u2{pkh(cs[0], cs[1]), pkh(cs[2], cs[3])};
    S2 = u2{pkh(sn[0], sn[1]), pkh(sn[2], sn[3])};
  }
  const u2 nS2 = S2 ^ 0x80008000u;
  const u4 A2re = cat(C2, S2), A2im = cat(nS2, C2);
  const u4 B3re = cat(C2, nS2), B3im = cat(S2, C2);
  u4 A4re, A4im, A4imn;
  {
    float cs[4], sn[4];
#pragma unroll
    for (int j = 0; j < 4; j++) {
      const float a = (float)((l15 * (4 * (4 * g4 + j) + k1)) & 63) * (1.0f / 64.0f);
      cs[j] = vcos(a) * 0.25f;
      sn[j] = vsin(a) * 0.25f;
    }
    const u2 C = u2{pkh(cs[0], cs[1]), pkh(cs[2], cs[3])}, Sg = u2{pkh(sn[0], sn[1]), pkh(sn[2], sn[3])};
    A4re = cat(C, Sg ^ 0x80008000u);
    A4im = cat(Sg, C);
    A4imn = A4im ^ 0x80008000u;
  }
  float t1c[4], t1s[4], t3c[4], t3s[4], pc[4], ps[4];
#pragma unroll
  for (int r = 0; r < 4; r++) {
    const float a1 = (float)((4 * g4 + r) * (4 * l15 + k1)) * (1.0f / 1024.0f);
    t1c[r] = vcos(a1); t1s[r] = -vsin(a1);
    const float a3 = (float)(l15 * (4 * (4 * g4 + r) + k1)) * (1.0f / 1024.0f);
    t3c[r] = vcos(a3); t3s[r] = vsin(a3);
    const float ph = (float)(k1 + 4 * l15 + 64 * (4 * g4 + r)) * (1.0f / 2048.0f);
    pc[r] = vcos(ph); ps[r] = vsin(ph);
  }
  const f4 z4 = {0, 0, 0, 0};
  f4 Zre[2], Zim[2];
#pragma unroll
  for (int u = 0; u < 2; u++) {
    u4 A;
#pragma unroll
    for (int i = 0; i < 4; i++) A[i] = u ? (pv[i][1] ^ 0x80000000u) : pv[i][0];
    const f4 pre = mfma_h(A, B1re, z4), pim = mfma_h(A, B1im, z4);
    float re[4], im[4];
#pragma unroll
    for (int r = 0; r < 4; r++) {
      re[r] = pre[r] * t1c[r] - pim[r] * t1s[r];
      im[r] = pre[r] * t1s[r] + pim[r] * t1c[r];
    }
    const u4 Bf = u4{pkh(re[0], re[1]), pkh(re[2], re[3]), pkh(im[0], im[1]), pkh(im[2], im[3])};
    Zre[u] = mfma_h(A2re, Bf, z4);
    Zim[u] = mfma_h(A2im, Bf, z4);
  }
  float fr[2][4], fi[2][4];
#pragma unroll
  for (int r = 0; r < 4; r++) {
    const u32 a = (r < 2) ? w0[2 * (r & 1)] : w1[2 * (r & 1)];
    const u32 c = (r < 2) ? w0[2 * (r & 1) + 1] : w1[2 * (r & 1) + 1];
    const float sr = 0.5f * (bflo(a) + bflo(c)), si = 0.5f * (bfhi(a) + bfhi(c));
    const float dr = 0.5f * (bflo(a) - bflo(c)), di = 0.5f * (bfhi(a) - bfhi(c));
    const float amr = sr - dr * ps[r], ami = si - di * ps[r];
    const float apr = sr + dr * ps[r], api = si + di * ps[r];
    const float br = dr * pc[r], bi = di * pc[r];
    const float zr = Zre[0][r], zi = Zim[0][r], yr = Zre[1][r], yi = Zim[1][r];
    fr[0][r] = amr * zr - ami * zi - (br * yi + bi * yr);
    fi[0][r] = amr * zi + ami * zr + (br * yr - bi * yi);
    fr[1][r] = apr * yr - api * yi + (br * zi + bi * zr);
    fi[1][r] = apr * yi + api * yr - (br * zr - bi * zi);
  }
  u32 outv[2][4];
#pragma unroll
  for (int u = 0; u < 2; u++) {
    const u4 A = u4{pkh(fr[u][0], fr[u][1]), pkh(fr[u][2], fr[u][3]), pkh(fi[u][0], fi[u][1]), pkh(fi[u][2], fi[u][3])};
    const f4 qre = mfma_h(A, B3re, z4), qim = mfma_h(A, B3im, z4);
    float re[4], im[4];
#pragma unroll
    for (int r = 0; r < 4; r++) {
      re[r] = qre[r] * t3c[r] - qim[r] * t3s[r];
      im[r] = qre[r] * t3s[r] + qim[r] * t3c[r];
    }
    const u4 Bf = u4{pkh(re[0], re[1]), pkh(re[2], re[3]), pkh(im[0], im[1]), pkh(im[2], im[3])};
    const f4 yre = mfma_h(A4re, Bf, z4);
    const f4 yim = mfma_h(u ? A4imn : A4im, Bf, z4);
#pragma unroll
    for (int r = 0; r < 4; r++) outv[u][r] = pkh(yre[r], yim[r]);
  }
#pragma unroll
  for (int r = 0; r < 4; r++) {
    const int s2 = 16 * (4 * g4 + r) + l15;
    *(u2*)(O + s2 * 8 + 2 * k1) = u2{outv[0][r], outv[1][r]};
  }
  __syncthreads();
  char* T2 = ws + WS_T2;
#pragma unroll
  for (int it = 0; it < 2; it++) {
    const int p = it * 256 + t;
    const u4 v = *(const u4*)(O + 4 * p);
    *(u4*)(T2 + (size_t)(p >> 1) * (ROWP * 4) + g * 32 + (p & 1) * 16) = v;
  }
}

__global__ __launch_bounds__(256) void k3_inv(const u16* __restrict__ x, char* __restrict__ ws) {
  __shared__ __attribute__((aligned(16))) char lds[ROWP * 4 + 18432 + 12288 + 2 * 10240];
  u32* Rw = (u32*)lds;
  char* CB = lds + ROWP * 4;
  char* EX = CB + 18432;
  char* SH = EX + 12288;
  char* SX = SH + 10240;
  const float* mod = (const float*)(ws + WS_MOD);
  const int s2 = xcd_map(blockIdx.x, 32);
  const int t = threadIdx.x, wv = t >> 6, l = t & 63, l15 = l & 15, g4 = l >> 4;
  const char* T2 = ws + WS_T2 + (size_t)s2 * (ROWP * 4);
  for (int p = t; p < 642; p += 256) *(u4*)(lds + p * 16) = *(const u4*)(T2 + p * 16);
  for (int p = t; p < 1152; p += 256) *(u4*)(CB + p * 16) = *(const u4*)(ws + WS_CB2 + p * 16);
  u2 xv[2][3];
  f4 gav[3], sbv[3], bv[3];
#pragma unroll
  for (int mt = 0; mt < 3; mt++) {
    const int d = 40 * l15 + offt(mt) + 4 * g4;
#pragma unroll
    for (int h = 0; h < 2; h++) {
      const int rr = 2 * wv + h;
      const int row = 2 * s2 + 512 * (rr >> 1) + (rr & 1);
      xv[h][mt] = *(const u2*)(x + (size_t)row * D_ + d);
    }
    gav[mt] = *(const f4*)(mod + 2 * 640 + d);
    sbv[mt] = *(const f4*)(mod + 3 * 640 + d);
    bv[mt] = *(const f4*)(mod + 4 * 640 + d);
  }
  const float unsc = *(const float*)(ws + WS_SCL);
  u4 Q0, Q1;
  {
    float v0[8], v1[8];
#pragma unroll
    for (int j = 0; j < 8; j++) {
      const int m = l15 * (4 * g4 + (j & 3)) + 4 * (j >> 2);
      v0[j] = vcos((float)(m & 15) * (1.0f / 16.0f)) * 0.125f;
      v1[j] = vcos((float)((m + 4) & 15) * (1.0f / 16.0f)) * 0.125f;
    }
    Q0 = u4{pkh(v0[0], v0[1]), pkh(v0[2], v0[3]), pkh(v0[4], v0[5]), pkh(v0[6], v0[7])};
    Q1 = u4{pkh(v1[0], v1[1]), pkh(v1[2], v1[3]), pkh(v1[4], v1[5]), pkh(v1[6], v1[7])};
  }
  float twc[3][4], tws[3][4];
#pragma unroll
  for (int ct = 0; ct < 3; ct++)
#pragma unroll
    for (int r = 0; r < 4; r++) {
      const float a = (float)((offt(ct) + l15) * (4 * g4 + r)) * (1.0f / 640.0f);
      twc[ct][r] = vcos(a);
      tws[ct][r] = vsin(a);
    }
  __syncthreads();
  const int k1 = wv;
  u4 Af[3];
#pragma unroll
  for (int kp = 0; kp < 3; kp++) {
#pragma unroll
    for (int i = 0; i < 4; i++) {
      int j2 = 16 * kp + 4 * g4 + i;
      j2 = j2 > 39 ? 39 : j2;
      const int kd = l15 + 16 * j2;
      const int pos = kd <= 320 ? 8 * kd + 2 * k1 : 8 * (640 - kd) + 2 * ((4 - k1) & 3) + 1;
      Af[kp][i] = Rw[pos];
    }
  }
#pragma unroll
  for (int ct = 0; ct < 3; ct++) {
    f4 ere = {0, 0, 0, 0}, eim = {0, 0, 0, 0};
#pragma unroll
    for (int kp = 0; kp < 3; kp++) {
      ere = mfma_h(Af[kp], *(const u4*)(CB + ((ct * 3 + kp) * 2 + 0) * 1024 + l * 16), ere);
      eim = mfma_h(Af[kp], *(const u4*)(CB + ((ct * 3 + kp) * 2 + 1) * 1024 + l * 16), eim);
    }
    float re[4], im[4];
#pragma unroll
    for (int r = 0; r < 4; r++) {
      re[r] = ere[r] * twc[ct][r] - eim[r] * tws[ct][r];
      im[r] = ere[r] * tws[ct][r] + eim[r] * twc[ct][r];
    }
    *(u4*)(EX + (k1 * 3 + ct) * 1024 + l * 16) =
        u4{pkh(re[0], re[1]), pkh(re[2], re[3]), pkh(im[0], im[1]), pkh(im[2], im[3])};
  }
  __syncthreads();
  const u4 Q2 = neg_h8(Q0), Q3 = neg_h8(Q1);
#pragma unroll
  for (int h = 0; h < 2; h++) {
    const int rr = 2 * wv + h, s1p = rr >> 1, par = rr & 1;
    f4 y[3];
#pragma unroll
    for (int mt = 0; mt < 3; mt++) {
      y[mt] = f4{0, 0, 0, 0};
#pragma unroll
      for (int kk = 0; kk < 4; kk++) {
        const int mu = (s1p * kk - par) & 3;
        const u4 B = mu == 0 ? Q0 : (mu == 1 ? Q1 : (mu == 2 ? Q2 : Q3));
        y[mt] = mfma_h(*(const u4*)(EX + (kk * 3 + mt) * 1024 + l * 16), B, y[mt]);
      }
    }
    float x1[3][4];
    float sum = 0.f;
    const bool valid2 = g4 >= 2;
#pragma unroll
    for (int mt = 0; mt < 3; mt++) {
#pragma unroll
      for (int r = 0; r < 4; r++) {
        const u32 xw = xv[h][mt][r >> 1];
        const float xx = (r & 1) ? bfhi(xw) : bflo(xw);
        const float yb = rbf(y[mt][r] * unsc);
        x1[mt][r] = rbf(xx + rbf(gav[mt][r] * yb));
        if (mt < 2 || valid2) sum += x1[mt][r];
      }
    }
    const float mean = wave_sum(sum) * (1.0f / 640.0f);
    float sq = 0.f;
#pragma unroll
    for (int mt = 0; mt < 3; mt++)
#pragma unroll
      for (int r = 0; r < 4; r++) {
        const float dlt = x1[mt][r] - mean;
        if (mt < 2 || valid2) sq += dlt * dlt;
      }
    const float rstd = rsqrtf(wave_sum(sq) * (1.0f / 640.0f) + 1e-5f);
#pragma unroll
    for (int mt = 0; mt < 3; mt++) {
      if (mt < 2 || valid2) {
        float hv[4];
#pragma unroll
        for (int r = 0; r < 4; r++) hv[r] = (x1[mt][r] - mean) * rstd * bv[mt][r] + sbv[mt][r];
        const int d = 40 * l15 + offt(mt) + 4 * g4;
        *(u2*)(SH + rr * 1280 + 2 * d) = u2{pkbf(hv[0], hv[1]), pkbf(hv[2], hv[3])};
        *(u2*)(SX + rr * 1280 + 2 * d) = u2{pkbf(x1[mt][0], x1[mt][1]), pkbf(x1[mt][2], x1[mt][3])};
      }
    }
  }
  __syncthreads();
  char* H2 = ws + WS_H2;
  char* X1 = ws + WS_X1;
  const int tile = s2 >> 1;
  for (int p = t; p < 640; p += 256) {
    const int rr = p & 7, gam = p >> 3;
    const u4 v = *(const u4*)(SH + rr * 1280 + gam * 16);
    *(u4*)(H2 + ((size_t)(tile * 80 + gam) * 16 + 8 * (s2 & 1) + rr) * 16) = v;
  }
  for (int p = t; p < 640; p += 256) {
    const int rr = p / 80, gam = p % 80;
    const u4 v = *(const u4*)(SX + rr * 1280 + gam * 16);
    *(u4*)(X1 + (size_t)(16 * tile + 8 * (s2 & 1) + rr) * 1280 + gam * 16) = v;
  }
}

__global__ __launch_bounds__(256) void k4_gemm1(char* __restrict__ ws) {
  extern __shared__ __attribute__((aligned(16))) char lds[];
  const int b = blockIdx.x;
  const int x = b & 7, r = b >> 3;
  const int tb = 4 * (x & 1) + (r & 3), j = 8 * (x >> 1) + (r >> 2);
  if (j >= NCH) return;
  const int t = threadIdx.x, wv = t >> 6, l = t & 63;
  const char* Wsrc = ws + WS_WGU + (size_t)j * 140 * 1024;
  const char* Hsrc = ws + WS_H2 + (size_t)(16 * tb + 4 * wv) * 20480 + l * 16;
  f4 acc[4][7];
#pragma unroll
  for (int m = 0; m < 4; m++)
#pragma unroll
    for (int i = 0; i < 7; i++) acc[m][i] = f4{0, 0, 0, 0};
  for (int p = wv; p < 140; p += 4) *(u4*)(lds + p * 1024 + l * 16) = *(const u4*)(Wsrc + p * 1024 + l * 16);
  __syncthreads();
#pragma unroll 2
  for (int kap = 0; kap < 20; kap++) {
    u4 tk[4];
#pragma unroll
    for (int m = 0; m < 4; m++) tk[m] = *(const u4*)(Hsrc + (size_t)m * 20480 + kap * 1024);
#pragma unroll
    for (int i = 0; i < 7; i++) {
      const u4 a = *(const u4*)(lds + (kap * 7 + i) * 1024 + l * 16);
#pragma unroll
      for (int m = 0; m < 4; m++) acc[m][i] = mfma_b(a, tk[m], acc[m][i]);
    }
  }
  char* TT = ws + WS_TT;
  const int c = l & 15, q = l >> 4;
#pragma unroll
  for (int i = 0; i < 7; i++) {
    u32 X[4];
#pragma unroll
    for (int m = 0; m < 4; m++) {
      const float g0 = rbf(acc[m][i][0]), g1 = rbf(acc[m][i][1]);
      const float u0 = rbf(acc[m][i][2]), u1 = rbf(acc[m][i][3]);
      X[m] = pkbf(g0 * u0, g1 * u1);
    }
    auto s0 = __builtin_amdgcn_permlane32_swap(X[0], X[2], false, false);
    auto s1 = __builtin_amdgcn_permlane32_swap(X[1], X[3], false, false);
    auto p0 = __builtin_amdgcn_permlane16_swap(s0[0], s1[0], false, false);
    auto p1 = __builtin_amdgcn_permlane16_swap(s0[1], s1[1], false, false);
    const u4 v = u4{p0[0], p0[1], p1[0], p1[1]};
    const int gam = 7 * j + i;
    if (gam < 216) *(u4*)(TT + ((size_t)(16 * tb + 4 * wv + q) * 216 + gam) * 256 + c * 16) = v;
  }
}

__global__ __launch_bounds__(256) void k5_gemm2(char* __restrict__ ws, u16* __restrict__ out) {
  extern __shared__ __attribute__((aligned(16))) char lds[];
  const int b = blockIdx.x;
  const int x = b & 7, r = b >> 3;
  const int tb = 8 * (x & 3) + (r & 7), cc = 4 * (x >> 2) + (r >> 3);
  const int t = threadIdx.x, wv = t >> 6, l = t & 63;
  const char* Wsrc = ws + WS_WD + (size_t)cc * 54 * 5 * 1024 + l * 16;
  const char* Tsrc = ws + WS_TT + (size_t)(4 * tb) * 216 * 256 + l * 16;
  f4 acc[4][5];
#pragma unroll
  for (int m = 0; m < 4; m++)
#pragma unroll
    for (int i = 0; i < 5; i++) acc[m][i] = f4{0, 0, 0, 0};
  const int s0 = 14 * wv, s1 = (s0 + 14 < 54) ? s0 + 14 : 54;
#pragma unroll 2
  for (int s = s0; s < s1; s++) {
    u4 tk[4], wf[5];
#pragma unroll
    for (int m = 0; m < 4; m++) tk[m] = *(const u4*)(Tsrc + (size_t)m * 216 * 256 + s * 1024);
#pragma unroll
    for (int i = 0; i < 5; i++) wf[i] = *(const u4*)(Wsrc + (s * 5 + i) * 1024);
#pragma unroll
    for (int i = 0; i < 5; i++)
#pragma unroll
      for (int m = 0; m < 4; m++) acc[m][i] = mfma_b(wf[i], tk[m], acc[m][i]);
  }
#pragma unroll
  for (int m = 0; m < 4; m++)
#pragma unroll
    for (int i = 0; i < 5; i++) *(f4*)(lds + wv * 20480 + (m * 5 + i) * 1024 + l * 16) = acc[m][i];
  __syncthreads();
  const float* mod = (const float*)(ws + WS_MOD);
  const char* X1 = ws + WS_X1;
#pragma unroll
  for (int it = 0; it < 5; it++) {
    const int item = it * 256 + t;
    const int tl = item >> 6, ln = item & 63;
    const int m = tl / 5, i = tl % 5, c = ln & 15, q = ln >> 4;
    f4 o = *(const f4*)(lds + item * 16);
#pragma unroll
    for (int w = 1; w < 4; w++) o += *(const f4*)(lds + w * 20480 + item * 16);
    const int d = 80 * cc + 16 * i + 4 * q;
    const int tile = 4 * tb + m;
    const f4 gb = *(const f4*)(mod + 5 * 640 + d);
    const u2 xw = *(const u2*)(X1 + (size_t)(16 * tile + c) * 1280 + 2 * d);
    float res[4];
#pragma unroll
    for (int e = 0; e < 4; e++) {
      const float xx = (e & 1) ? bfhi(xw[e >> 1]) : bflo(xw[e >> 1]);
      res[e] = xx + rbf(gb[e] * rbf(o[e]));
    }
    *(u2*)(out + (size_t)tok_row(tile, c) * D_ + d) = u2{pkbf(res[0], res[1]), pkbf(res[2], res[3])};
  }
}

extern "C" void kernel_launch(void* const* d_in, const int* in_sizes, int n_in, void* d_out, int out_size, void* d_ws,
                              size_t ws_size, hipStream_t stream) {
  const u16* x = (const u16*)d_in[0];
  const u16* wr = (const u16*)d_in[1];
  const u16* wi = (const u16*)d_in[2];
  const u16* wg = (const u16*)d_in[4];
  const u16* wu = (const u16*)d_in[5];
  const u16* wd = (const u16*)d_in[6];
  const u16* c = (const u16*)d_in[7];
  const u16* wmod = (const u16*)d_in[8];
  char* ws = (char*)d_ws;
  (void)hipFuncSetAttribute((const void*)k4_gemm1, hipFuncAttributeMaxDynamicSharedMemorySize, 140 * 1024);
  (void)hipFuncSetAttribute((const void*)k5_gemm2, hipFuncAttributeMaxDynamicSharedMemorySize, 80 * 1024);
  k0_prep<<<K0_B6, 256, 0, stream>>>(c, wmod, wr, wi, wg, wu, wd, ws);
  k1_fwd<<<256, 256, 0, stream>>>(x, ws);
  k2_filter<<<321, 256, 0, stream>>>(ws);
  k3_inv<<<256, 256, 0, stream>>>(x, ws);
  k4_gemm1<<<256, 256, 140 * 1024, stream>>>(ws);
  k5_gemm2<<<256, 256, 80 * 1024, stream>>>(ws, (u16*)d_out);
}
```

```cpp
#include <hip/hip_runtime.h>
#include <stdint.h>

typedef unsigned int u32;
typedef unsigned short u16;
typedef _Float16 h2 __attribute__((ext_vector_type(2)));
typedef _Float16 h8 __attribute__((ext_vector_type(8)));
typedef __bf16 b2 __attribute__((ext_vector_type(2)));
typedef __bf16 b8 __attribute__((ext_vector_type(8)));
typedef float f2 __attribute__((ext_vector_type(2)));
typedef float f4 __attribute__((ext_vector_type(4)));
typedef float f16v __attribute__((ext_vector_type(16)));
typedef u32 u2 __attribute__((ext_vector_type(2)));
typedef u32 u4 __attribute__((ext_vector_type(4)));
typedef short s4 __attribute__((ext_vector_type(4)));
typedef __attribute__((address_space(3))) s4* lds_s4p;
typedef u4 u4a2 __attribute__((aligned(2)));
#define DI __device__ __forceinline__
#define SB() __builtin_amdgcn_sched_barrier(0)
#define PIN(x) asm volatile("" : "+v"(x))

constexpr int S_ = 2048, D_ = 640, F_ = 1728, DF_ = 321;
constexpr int ROWP = 2568;

constexpr size_t WS_MOD = 0;
constexpr size_t WS_MODP = 16384 + 64;
constexpr size_t WS_SCL = 16384;
constexpr size_t WS_CNT = 32768;
constexpr size_t WS_T1 = 65536;
constexpr size_t SZ_T = (size_t)321 * 256 * 32;
constexpr size_t WS_T2 = WS_T1 + SZ_T;
constexpr size_t WS_WT = WS_T2 + SZ_T;
constexpr size_t WS_H2 = WS_WT + (size_t)321 * 8192;
constexpr int HST = 84 * 256;
constexpr int TST = 220 * 256;
constexpr size_t WS_X1 = WS_H2 + (size_t)128 * HST;
constexpr size_t WS_TT = WS_X1 + (size_t)S_ * D_ * 2;
constexpr size_t WS_WD = WS_TT + (size_t)128 * TST;
constexpr size_t WS_END = WS_WD + (size_t)8 * 54 * 5 * 1024;

__device__ __attribute__((aligned(16))) const unsigned int CA2_TAB[3840] = {
0x310f310f,0x310f310f,0x0,0x0,0x80008000,0x80008000,0x310f310f,0x310f310f,0x30ff310f,0x308230d0,0x26550000,0x2c982a41,
0xa6558000,0xac98aa41,0x30ff310f,0x308230d0,0x30d0310f,0x2df33018,0x2a410000,0x30182df3,0xaa418000,0xb018adf3,0x30d0310f,0x2df33018,
0x3082310f,0x26552df3,0x2c980000,0x30ff3018,0xac988000,0xb0ffb018,0x3082310f,0x26552df3,0x3018310f,0xaa412a41,0x2df30000,0x30d030d0,
0xadf38000,0xb0d0b0d0,0x3018310f,0xaa412a41,0x2f28310f,0xaf280000,0x2f280000,0x2f28310f,0xaf288000,0xaf28b10f,0x2f28310f,0xaf280000,
0x2df3310f,0xb0d0aa41,0x30180000,0x2a4130d0,0xb0188000,0xaa41b0d0,0x2df3310f,0xb0d0aa41,0x2c98310f,0xb0ffadf3,0x30820000,0xa6553018,
0xb0828000,0x2655b018,0x2c98310f,0xb0ffadf3,0x310f310f,0x310f310f,0x0,0x0,0x80008000,0x80008000,0x310f310f,0x310f310f,
0x2f283018,0x2c982df3,0x2f282df3,0x30823018,0xaf28adf3,0xb082b018,0x2f283018,0x2c982df3,0x2a41,0xadf3aa41,0x310f30d0,0x301830d0,
0xb10fb0d0,0xb018b0d0,0x2a41,0xadf3aa41,0xaf28aa41,0xb0ffb0d0,0x2f2830d0,0xa6552a41,0xaf28b0d0,0x2655aa41,0xaf28aa41,0xb0ffb0d0,
0xb10fb018,0xaa41b018,0x2df3,0xb0d0adf3,0x8000adf3,0x30d02df3,0xb10fb018,0xaa41b018,0xaf28b10f,0x2f288000,0xaf280000,0xaf28b10f,
0x2f288000,0x2f28310f,0xaf28b10f,0x2f288000,0x8000b018,0x30d03018,0xb10fadf3,0x2a41adf3,0x310f2df3,0xaa412df3,0x8000b018,0x30d03018,
0x2f28aa41,0x265530d0,0xaf28b0d0,0x30ff2a41,0x2f2830d0,0xb0ffaa41,0x2f28aa41,0x265530d0,0x310f310f,0x310f310f,0x0,0x0,
0x80008000,0x80008000,0x310f310f,0x310f310f,0x26552a41,0xa6550000,0x30ff30d0,0x30ff310f,0xb0ffb0d0,0xb0ffb10f,0x26552a41,0xa6550000,
0xb0d0b018,0xb0d0b10f,0x2a412df3,0xaa410000,0xaa41adf3,0x2a418000,0xb0d0b018,0xb0d0b10f,0xac98b018,0x2c988000,0xb082adf3,0xb082b10f,
0x30822df3,0x3082310f,0xac98b018,0x2c988000,0x30182a41,0x3018310f,0xadf3b0d0,0x2df30000,0x2df330d0,0xadf38000,0x30182a41,0x3018310f,
0x2f28310f,0xaf280000,0x2f280000,0x2f28310f,0xaf288000,0xaf28b10f,0x2f28310f,0xaf280000,0xadf32a41,0xadf3b10f,0x301830d0,0xb0180000,
0xb018b0d0,0x30188000,0xadf32a41,0xadf3b10f,0xb082b018,0x30828000,0xac982df3,0xac98b10f,0x2c98adf3,0x2c98310f,0xb082b018,0x30828000,
0x310f310f,0x310f310f,0x0,0x0,0x80008000,0x80008000,0x310f310f,0x310f310f,0xac98aa41,0xaf28adf3,0x308230d0,0x2f283018,
0xb082b0d0,0xaf28b018,0xac98aa41,0xaf28adf3,0xadf3b018,0x8000aa41,0xb018adf3,0xb10fb0d0,0x30182df3,0x310f30d0,0xadf3b018,0x8000aa41,
0x30ff3018,0x2f2830d0,0xa655adf3,0x2f282a41,0x26552df3,0xaf28aa41,0x30ff3018,0x2f2830d0,0xaa412a41,0xb10fb018,0x30d030d0,0x2df3,
0xb0d0b0d0,0x8000adf3,0xaa412a41,0xb10fb018,0xaf28b10f,0x2f288000,0xaf280000,0xaf28b10f,0x2f288000,0x2f28310f,0xaf28b10f,0x2f288000,
0x30d02a41,0x3018,0xaa41b0d0,0x310f2df3,0x2a4130d0,0xb10fadf3,0x30d02a41,0x3018,0xa6553018,0xaf28b0d0,0x30ff2df3,0xaf282a41,
0xb0ffadf3,0x2f28aa41,0xa6553018,0xaf28b0d0,0x310f310f,0x310f310f,0x0,0x0,0x80008000,0x80008000,0x310f310f,0x310f310f,
0xb082b018,0xb0ffb0d0,0x2c982df3,0x26552a41,0xac98adf3,0xa655aa41,0xb082b018,0xb0ffb0d0,0x2df32a41,0x30d03018,0xb018b0d0,0xaa41adf3,
0x301830d0,0x2a412df3,0x2df32a41,0x30d03018,0xa6552a41,0xb082adf3,0x30ff30d0,0x2c983018,0xb0ffb0d0,0xac98b018,0xa6552a41,0xb082adf3,
0xaa41b018,0x30182a41,0xb0d0adf3,0xadf3b0d0,0x30d02df3,0x2df330d0,0xaa41b018,0x30182a41,0x2f28310f,0xaf280000,0x2f280000,0x2f28310f,
0xaf288000,0xaf28b10f,0x2f28310f,0xaf280000,0xb0d0b018,0x2df3aa41,0xaa412df3,0xb018b0d0,0x2a41adf3,0x301830d0,0xb0d0b018,0x2df3aa41,
0x30ff2a41,0xac982df3,0xa655b0d0,0x30823018,0x265530d0,0xb082b018,0x30ff2a41,0xac982df3,0x310f310f,0x310f310f,0x0,0x0,
0x80008000,0x80008000,0x310f310f,0x310f310f,0xb0ffb10f,0xb082b0d0,0xa6550000,0xac98aa41,0x26558000,0x2c982a41,0xb0ffb10f,0xb082b0d0,
0x30d0310f,0x2df33018,0x2a410000,0x30182df3,0xaa418000,0xb018adf3,0x30d0310f,0x2df33018,0xb082b10f,0xa655adf3,0xac980000,0xb0ffb018,
0x2c988000,0x30ff3018,0xb082b10f,0xa655adf3,0x3018310f,0xaa412a41,0x2df30000,0x30d030d0,0xadf38000,0xb0d0b0d0,0x3018310f,0xaa412a41,
0xaf28b10f,0x2f288000,0xaf280000,0xaf28b10f,0x2f288000,0x2f28310f,0xaf28b10f,0x2f288000,0x2df3310f,0xb0d0aa41,0x30180000,0x2a4130d0,
0xb0188000,0xaa41b0d0,0x2df3310f,0xb0d0aa41,0xac98b10f,0x30ff2df3,0xb0820000,0x2655b018,0x30828000,0xa6553018,0xac98b10f,0x30ff2df3,
0x310f310f,0x310f310f,0x0,0x0,0x80008000,0x80008000,0x310f310f,0x310f310f,0xaf28b018,0xac98adf3,0xaf28adf3,0xb082b018,
0x2f282df3,0x30823018,0xaf28b018,0xac98adf3,0x2a41,0xadf3aa41,0x310f30d0,0x301830d0,0xb10fb0d0,0xb018b0d0,0x2a41,0xadf3aa41,
0x2f282a41,0x30ff30d0,0xaf28b0d0,0x2655aa41,0x2f2830d0,0xa6552a41,0x2f282a41,0x30ff30d0,0xb10fb018,0xaa41b018,0x2df3,0xb0d0adf3,
0x8000adf3,0x30d02df3,0xb10fb018,0xaa41b018,0x2f28310f,0xaf280000,0x2f280000,0x2f28310f,0xaf288000,0xaf28b10f,0x2f28310f,0xaf280000,
0x8000b018,0x30d03018,0xb10fadf3,0x2a41adf3,0x310f2df3,0xaa412df3,0x8000b018,0x30d03018,0xaf282a41,0xa655b0d0,0x2f2830d0,0xb0ffaa41,
0xaf28b0d0,0x30ff2a41,0xaf282a41,0xa655b0d0,0x310f310f,0x310f310f,0x0,0x0,0x80008000,0x80008000,0x310f310f,0x310f310f,
0xa655aa41,0x26558000,0xb0ffb0d0,0xb0ffb10f,0x30ff30d0,0x30ff310f,0xa655aa41,0x26558000,0xb0d0b018,0xb0d0b10f,0x2a412df3,0xaa410000,
0xaa41adf3,0x2a418000,0xb0d0b018,0xb0d0b10f,0x2c983018,0xac980000,0x30822df3,0x3082310f,0xb082adf3,0xb082b10f,0x2c983018,0xac980000,
0x30182a41,0x3018310f,0xadf3b0d0,0x2df30000,0x2df330d0,0xadf38000,0x30182a41,0x3018310f,0xaf28b10f,0x2f288000,0xaf280000,0xaf28b10f,
0x2f288000,0x2f28310f,0xaf28b10f,0x2f288000,0xadf32a41,0xadf3b10f,0x301830d0,0xb0180000,0xb018b0d0,0x30188000,0xadf32a41,0xadf3b10f,
0x30823018,0xb0820000,0x2c98adf3,0x2c98310f,0xac982df3,0xac98b10f,0x30823018,0xb0820000,0x0,0x0,0x0,0x0,
0x0,0x0,0x0,0x0,0x0,0x0,0x0,0x0,0x0,0x0,0x0,0x0,
0x0,0x0,0x0,0x0,0x0,0x0,0x0,0x0,0x0,0x0,0x0,0x0,
0x0,0x0,0x0,0x0,0x0,0x0,0x0,0x0,0x0,0x0,0x0,0x0,
0x0,0x0,0x0,0x0,0x0,0x0,0x0,0x0,0x0,0x0,0x0,0x0,
0x0,0x0,0x0,0x0,0x0,0x0,0x0,0x0,0x0,0x0,0x0,0x0,
0x0,0x0,0x0,0x0,0x0,0x0,0x0,0x0,0x0,0x0,0x0,0x0,
0x0,0x0,0x0,0x0,0x0,0x0,0x0,0x0,0x0,0x0,0x0,0x0,
0x0,0x0,0x0,0x0,0x0,0x0,0x0,0x0,0x0,0x0,0x0,0x0,
0x0,0x0,0x0,0x0,0x0,0x0,0x0,0x0,0x0,0x0,0x0,0x0,
0x0,0x0,0x0,0x0,0x0,0x0,0x0,0x0,0x0,0x0,0x0,0x0,
0x0,0x0,0x0,0x0,0x310f310f,0x310f310f,0x0,0x0,0x80008000,0x80008000,0x310f310f,0x310f310f,
0x2c982a41,0x2f282df3,0xb082b0d0,0xaf28b018,0x308230d0,0x2f283018,0x2c982a41,0x2f282df3,0xadf3b018,0x8000aa41,0xb018adf3,0xb10fb0d0,
0x30182df3,0x310f30d0,0xadf3b018,0x8000aa41,0xb0ffb018,0xaf28b0d0,0x26552df3,0xaf28aa41,0xa655adf3,0x2f282a41,0xb0ffb018,0xaf28b0d0,
0xaa412a41,0xb10fb018,0x30d030d0,0x2df3,0xb0d0b0d0,0x8000adf3,0xaa412a41,0xb10fb018,0x2f28310f,0xaf280000,0x2f280000,0x2f28310f,
0xaf288000,0xaf28b10f,0x2f28310f,0xaf280000,0x30d02a41,0x3018,0xaa41b0d0,0x310f2df3,0x2a4130d0,0xb10fadf3,0x30d02a41,0x3018,
0x2655b018,0x2f2830d0,0xb0ffadf3,0x2f28aa41,0x30ff2df3,0xaf282a41,0x2655b018,0x2f2830d0,0x310f310f,0x310f310f,0x0,0x0,
0x80008000,0x80008000,0x310f310f,0x310f310f,0x30823018,0x30ff30d0,0xac98adf3,0xa655aa41,0x2c982df3,0x26552a41,0x30823018,0x30ff30d0,
0x2df32a41,0x30d03018,0xb018b0d0,0xaa41adf3,0x301830d0,0x2a412df3,0x2df32a41,0x30d03018,0x2655aa41,0x30822df3,0xb0ffb0d0,0xac98b018,
0x30ff30d0,0x2c983018,0x2655aa41,0x30822df3,0xaa41b018,0x30182a41,0xb0d0adf3,0xadf3b0d0,0x30d02df3,0x2df330d0,0xaa41b018,0x30182a41,
0xaf28b10f,0x2f288000,0xaf280000,0xaf28b10f,0x2f288000,0x2f28310f,0xaf28b10f,0x2f288000,0xb0d0b018,0x2df3aa41,0xaa412df3,0xb018b0d0,
0x2a41adf3,0x301830d0,0xb0d0b018,0x2df3aa41,0xb0ffaa41,0x2c98adf3,0x265530d0,0xb082b018,0xa655b0d0,0x30823018,0xb0ffaa41,0x2c98adf3,
0x2a41310f,0xb018b018,0x30d00000,0xadf32df3,0xb0d08000,0x2df3adf3,0x2a41310f,0xb018b018,0x2655310f,0xac98b0d0,0x30ff0000,0xb0822a41,
0xb0ff8000,0x3082aa41,0x2655310f,0xac98b0d0,0x310f,0x8000b10f,0x310f0000,0xb10f0000,0xb10f8000,0x310f8000,0x310f,0x8000b10f,
0xa655310f,0x2c98b0d0,0x30ff0000,0xb082aa41,0xb0ff8000,0x30822a41,0xa655310f,0x2c98b0d0,0xaa41310f,0x3018b018,0x30d00000,0xadf3adf3,
0xb0d08000,0x2df32df3,0xaa41310f,0x3018b018,0xac98310f,0x30ffadf3,0x30820000,0xa655b018,0xb0828000,0x26553018,0xac98310f,0x30ffadf3,
0xadf3310f,0x30d0aa41,0x30180000,0x2a41b0d0,0xb0188000,0xaa4130d0,0xadf3310f,0x30d0aa41,0xaf28310f,0x2f288000,0x2f280000,0x2f28b10f,
0xaf288000,0xaf28310f,0xaf28310f,0x2f288000,0x310f2a41,0xb0182a41,0xb0d0,0x2df330d0,0x800030d0,0xadf3b0d0,0x310f2a41,0xb0182a41,
0x2f283018,0xb082adf3,0x2f28adf3,0xac983018,0xaf282df3,0x2c98b018,0x2f283018,0xb082adf3,0x310f,0x8000b10f,0x310f0000,0xb10f0000,
0xb10f8000,0x310f8000,0x310f,0x8000b10f,0xaf283018,0x3082adf3,0x2f282df3,0xac98b018,0xaf28adf3,0x2c983018,0xaf283018,0x3082adf3,
0xb10f2a41,0x30182a41,0x30d0,0x2df3b0d0,0x8000b0d0,0xadf330d0,0xb10f2a41,0x30182a41,0xaf28aa41,0xa65530d0,0xaf2830d0,0x30ffaa41,
0x2f28b0d0,0xb0ff2a41,0xaf28aa41,0xa65530d0,0x8000b018,0xb0d03018,0xb10f2df3,0x2a412df3,0x310fadf3,0xaa41adf3,0x8000b018,0xb0d03018,
0x2f28b10f,0xaf280000,0xaf280000,0xaf28310f,0x2f288000,0x2f28b10f,0x2f28b10f,0xaf280000,0x2a41b018,0x2a41310f,0xb0d0adf3,0x30d00000,
0x30d02df3,0xb0d08000,0x2a41b018,0x2a41310f,0x30ff2a41,0xb0ff0000,0x2655b0d0,0x2655310f,0xa65530d0,0xa655b10f,0x30ff2a41,0xb0ff0000,
0x310f,0x8000b10f,0x310f0000,0xb10f0000,0xb10f8000,0x310f8000,0x310f,0x8000b10f,0xb0ff2a41,0x30ff8000,0x265530d0,0x2655b10f,
0xa655b0d0,0xa655310f,0xb0ff2a41,0x30ff8000,0xaa41b018,0xaa41310f,0xb0d02df3,0x30d00000,0x30d0adf3,0xb0d08000,0xaa41b018,0xaa41310f,
0x3082b018,0xb0820000,0xac98adf3,0xac98310f,0x2c982df3,0x2c98b10f,0x3082b018,0xb0820000,0x2df32a41,0x2df3b10f,0x3018b0d0,0xb0180000,
0xb01830d0,0x30188000,0x2df32a41,0x2df3b10f,0xaf28310f,0x2f288000,0x2f280000,0x2f28b10f,0xaf288000,0xaf28310f,0xaf28310f,0x2f288000,
0xb018b018,0x310f2a41,0xadf32df3,0xb0d0,0x2df3adf3,0x800030d0,0xb018b018,0x310f2a41,0x3082aa41,0xaf282df3,0xac98b0d0,0x2f283018,
0x2c9830d0,0xaf28b018,0x3082aa41,0xaf282df3,0x310f,0x8000b10f,0x310f0000,0xb10f0000,0xb10f8000,0x310f8000,0x310f,0x8000b10f,
0xb082aa41,0x2f282df3,0xac9830d0,0x2f28b018,0x2c98b0d0,0xaf283018,0xb082aa41,0x2f282df3,0x3018b018,0xb10f2a41,0xadf3adf3,0x30d0,
0x2df32df3,0x8000b0d0,0x3018b018,0xb10f2a41,0x26553018,0x2f28b0d0,0x30ffadf3,0xaf28aa41,0xb0ff2df3,0x2f282a41,0x26553018,0x2f28b0d0,
0xb0d02a41,0x3018,0xaa4130d0,0x310fadf3,0x2a41b0d0,0xb10f2df3,0xb0d02a41,0x3018,0x2f28b10f,0xaf280000,0xaf280000,0xaf28310f,
0x2f288000,0x2f28b10f,0x2f28b10f,0xaf280000,0xb0182a41,0x2a41b018,0x2df330d0,0xb0d0adf3,0xadf3b0d0,0x30d02df3,0xb0182a41,0x2a41b018,
0x2c98b018,0xa65530d0,0xb082adf3,0x30ff2a41,0x30822df3,0xb0ffaa41,0x2c98b018,0xa65530d0,0x310f,0x8000b10f,0x310f0000,0xb10f0000,
0xb10f8000,0x310f8000,0x310f,0x8000b10f,0xac98b018,0x265530d0,0xb0822df3,0x30ffaa41,0x3082adf3,0xb0ff2a41,0xac98b018,0x265530d0,
0x30182a41,0xaa41b018,0x2df3b0d0,0xb0d02df3,0xadf330d0,0x30d0adf3,0x30182a41,0xaa41b018,0xb0ff2a41,0x2c982df3,0xa65530d0,0x3082b018,
0x2655b0d0,0xb0823018,0xb0ff2a41,0x2c982df3,0x30d0b018,0xadf3aa41,0xaa41adf3,0xb01830d0,0x2a412df3,0x3018b0d0,0x30d0b018,0xadf3aa41,
0xaf28310f,0x2f288000,0x2f280000,0x2f28b10f,0xaf288000,0xaf28310f,0xaf28310f,0x2f288000,0x2a41310f,0xb018b018,0x30d00000,0xadf32df3,
0xb0d08000,0x2df3adf3,0x2a41310f,0xb018b018,0xa655b10f,0x2c9830d0,0xb0ff0000,0x3082aa41,0x30ff8000,0xb0822a41,0xa655b10f,0x2c9830d0,
0x310f,0x8000b10f,0x310f0000,0xb10f0000,0xb10f8000,0x310f8000,0x310f,0x8000b10f,0x2655b10f,0xac9830d0,0xb0ff0000,0x30822a41,
0x30ff8000,0xb082aa41,0x2655b10f,0xac9830d0,0xaa41310f,0x3018b018,0x30d00000,0xadf3adf3,0xb0d08000,0x2df32df3,0xaa41310f,0x3018b018,
0x2c98b10f,0xb0ff2df3,0xb0820000,0x26553018,0x30828000,0xa655b018,0x2c98b10f,0xb0ff2df3,0xadf3310f,0x30d0aa41,0x30180000,0x2a41b0d0,
0xb0188000,0xaa4130d0,0xadf3310f,0x30d0aa41,0x2f28b10f,0xaf280000,0xaf280000,0xaf28310f,0x2f288000,0x2f28b10f,0x2f28b10f,0xaf280000,
0x310f2a41,0xb0182a41,0xb0d0,0x2df330d0,0x800030d0,0xadf3b0d0,0x310f2a41,0xb0182a41,0xaf28b018,0x30822df3,0xaf282df3,0x2c98b018,
0x2f28adf3,0xac983018,0xaf28b018,0x30822df3,0x310f,0x8000b10f,0x310f0000,0xb10f0000,0xb10f8000,0x310f8000,0x310f,0x8000b10f,
0x2f28b018,0xb0822df3,0xaf28adf3,0x2c983018,0x2f282df3,0xac98b018,0x2f28b018,0xb0822df3,0xb10f2a41,0x30182a41,0x30d0,0x2df3b0d0,
0x8000b0d0,0xadf330d0,0xb10f2a41,0x30182a41,0x2f282a41,0x2655b0d0,0x2f28b0d0,0xb0ff2a41,0xaf2830d0,0x30ffaa41,0x2f282a41,0x2655b0d0,
0x8000b018,0xb0d03018,0xb10f2df3,0x2a412df3,0x310fadf3,0xaa41adf3,0x8000b018,0xb0d03018,0xaf28310f,0x2f288000,0x2f280000,0x2f28b10f,
0xaf288000,0xaf28310f,0xaf28310f,0x2f288000,0x2a41b018,0x2a41310f,0xb0d0adf3,0x30d00000,0x30d02df3,0xb0d08000,0x2a41b018,0x2a41310f,
0xb0ffaa41,0x30ff8000,0xa65530d0,0xa655b10f,0x2655b0d0,0x2655310f,0xb0ffaa41,0x30ff8000,0x310f,0x8000b10f,0x310f0000,0xb10f0000,
0xb10f8000,0x310f8000,0x310f,0x8000b10f,0x30ffaa41,0xb0ff0000,0xa655b0d0,0xa655310f,0x265530d0,0x2655b10f,0x30ffaa41,0xb0ff0000,
0xaa41b018,0xaa41310f,0xb0d02df3,0x30d00000,0x30d0adf3,0xb0d08000,0xaa41b018,0xaa41310f,0xb0823018,0x30828000,0x2c982df3,0x2c98b10f,
0xac98adf3,0xac98310f,0xb0823018,0x30828000,0x2df32a41,0x2df3b10f,0x3018b0d0,0xb0180000,0xb01830d0,0x30188000,0x2df32a41,0x2df3b10f,
0x2f28b10f,0xaf280000,0xaf280000,0xaf28310f,0x2f288000,0x2f28b10f,0x2f28b10f,0xaf280000,0x0,0x0,0x0,0x0,
0x0,0x0,0x0,0x0,0x0,0x0,0x0,0x0,0x0,0x0,0x0,0x0,
0x0,0x0,0x0,0x0,0x0,0x0,0x0,0x0,0x0,0x0,0x0,0x0,
0x0,0x0,0x0,0x0,0x0,0x0,0x0,0x0,0x0,0x0,0x0,0x0,
0x0,0x0,0x0,0x0,0x0,0x0,0x0,0x0,0x0,0x0,0x0,0x0,
0x0,0x0,0x0,0x0,0x0,0x0,0x0,0x0,0x0,0x0,0x0,0x0,
0x0,0x0,0x0,0x0,0x0,0x0,0x0,0x0,0x0,0x0,0x0,0x0,
0x0,0x0,0x0,0x0,0x0,0x0,0x0,0x0,0x0,0x0,0x0,0x0,
0x0,0x0,0x0,0x0,0x0,0x0,0x0,0x0,0x0,0x0,0x0,0x0,
0x0,0x0,0x0,0x0,0x0,0x0,0x0,0x0,0x0,0x0,0x0,0x0,
0x0,0x0,0x0,0x0,0x0,0x0,0x0,0x0,0x0,0x0,0x0,0x0,
0x0,0x0,0x0,0x0,0xb018b018,0x310f2a41,0xadf32df3,0xb0d0,0x2df3adf3,0x800030d0,0xb018b018,0x310f2a41,
0xb0822a41,0x2f28adf3,0x2c9830d0,0xaf28b018,0xac98b0d0,0x2f283018,0xb0822a41,0x2f28adf3,0x310f,0x8000b10f,0x310f0000,0xb10f0000,
0xb10f8000,0x310f8000,0x310f,0x8000b10f,0x30822a41,0xaf28adf3,0x2c98b0d0,0xaf283018,0xac9830d0,0x2f28b018,0x30822a41,0xaf28adf3,
0x3018b018,0xb10f2a41,0xadf3adf3,0x30d0,0x2df32df3,0x8000b0d0,0x3018b018,0xb10f2a41,0xa655b018,0xaf2830d0,0xb0ff2df3,0x2f282a41,
0x30ffadf3,0xaf28aa41,0xa655b018,0xaf2830d0,0xb0d02a41,0x3018,0xaa4130d0,0x310fadf3,0x2a41b0d0,0xb10f2df3,0xb0d02a41,0x3018,
0xaf28310f,0x2f288000,0x2f280000,0x2f28b10f,0xaf288000,0xaf28310f,0xaf28310f,0x2f288000,0xb0182a41,0x2a41b018,0x2df330d0,0xb0d0adf3,
0xadf3b0d0,0x30d02df3,0xb0182a41,0x2a41b018,0xac983018,0x2655b0d0,0x30822df3,0xb0ffaa41,0xb082adf3,0x30ff2a41,0xac983018,0x2655b0d0,
0x310f,0x8000b10f,0x310f0000,0xb10f0000,0xb10f8000,0x310f8000,0x310f,0x8000b10f,0x2c983018,0xa655b0d0,0x3082adf3,0xb0ff2a41,
0xb0822df3,0x30ffaa41,0x2c983018,0xa655b0d0,0x30182a41,0xaa41b018,0x2df3b0d0,0xb0d02df3,0xadf330d0,0x30d0adf3,0x30182a41,0xaa41b018,
0x30ffaa41,0xac98adf3,0x2655b0d0,0xb0823018,0xa65530d0,0x3082b018,0x30ffaa41,0xac98adf3,0x30d0b018,0xadf3aa41,0xaa41adf3,0xb01830d0,
0x2a412df3,0x3018b0d0,0x30d0b018,0xadf3aa41,0x2f28b10f,0xaf280000,0xaf280000,0xaf28310f,0x2f288000,0x2f28b10f,0x2f28b10f,0xaf280000,
0xb018310f,0x2a412a41,0x2df30000,0x30d0b0d0,0xadf38000,0xb0d030d0,0xb018310f,0x2a412a41,0xb082310f,0xa6552df3,0x2c980000,0x30ffb018,
0xac988000,0xb0ff3018,0xb082310f,0xa6552df3,0xb0d0310f,0xadf33018,0x2a410000,0x3018adf3,0xaa418000,0xb0182df3,0xb0d0310f,0xadf33018,
0xb0ff310f,0xb08230d0,0x26550000,0x2c98aa41,0xa6558000,0xac982a41,0xb0ff310f,0xb08230d0,0xb10f310f,0xb10f310f,0x0,0x0,
0x80008000,0x80008000,0xb10f310f,0xb10f310f,0xb0ff310f,0xb08230d0,0xa6550000,0xac982a41,0x26558000,0x2c98aa41,0xb0ff310f,0xb08230d0,
0xb0d0310f,0xadf33018,0xaa410000,0xb0182df3,0x2a418000,0x3018adf3,0xb0d0310f,0xadf33018,0xb082310f,0xa6552df3,0xac980000,0xb0ff3018,
0x2c988000,0x30ffb018,0xb082310f,0xa6552df3,0x310fb018,0x2a41b018,0xadf3,0xb0d02df3,0x80002df3,0x30d0adf3,0x310fb018,0x2a41b018,
0x2f28aa41,0x30ffb0d0,0x2f28b0d0,0xa655aa41,0xaf2830d0,0x26552a41,0x2f28aa41,0x30ffb0d0,0x2a41,0x2df3aa41,0x310fb0d0,0x3018b0d0,
0xb10f30d0,0xb01830d0,0x2a41,0x2df3aa41,0xaf283018,0xac982df3,0x2f28adf3,0x3082b018,0xaf282df3,0xb0823018,0xaf283018,0xac982df3,
0xb10f310f,0xb10f310f,0x0,0x0,0x80008000,0x80008000,0xb10f310f,0xb10f310f,0xaf283018,0xac982df3,0xaf282df3,0xb0823018,
0x2f28adf3,0x3082b018,0xaf283018,0xac982df3,0x80002a41,0x2df3aa41,0xb10f30d0,0xb01830d0,0x310fb0d0,0x3018b0d0,0x80002a41,0x2df3aa41,
0x2f28aa41,0x30ffb0d0,0xaf2830d0,0x26552a41,0x2f28b0d0,0xa655aa41,0x2f28aa41,0x30ffb0d0,0xb0182a41,0xb018310f,0xadf330d0,0x2df30000,
0x2df3b0d0,0xadf38000,0xb0182a41,0xb018310f,0x2c98b018,0xac980000,0xb0822df3,0xb082310f,0x3082adf3,0x3082b10f,0x2c98b018,0xac980000,
0x30d0b018,0x30d0b10f,0x2a41adf3,0xaa410000,0xaa412df3,0x2a418000,0x30d0b018,0x30d0b10f,0xa6552a41,0x26558000,0x30ffb0d0,0x30ffb10f,
0xb0ff30d0,0xb0ff310f,0xa6552a41,0x26558000,0xb10f310f,0xb10f310f,0x0,0x0,0x80008000,0x80008000,0xb10f310f,0xb10f310f,
0xa6552a41,0x26550000,0xb0ff30d0,0xb0ff310f,0x30ffb0d0,0x30ffb10f,0xa6552a41,0x26550000,0x30d0b018,0x30d0b10f,0xaa412df3,0x2a410000,
0x2a41adf3,0xaa418000,0x30d0b018,0x30d0b10f,0x2c98b018,0xac988000,0x3082adf3,0x3082b10f,0xb0822df3,0xb082310f,0x2c98b018,0xac988000,
0x2a412a41,0x310fb018,0x30d0b0d0,0xadf3,0xb0d030d0,0x80002df3,0x2a412a41,0x310fb018,0xb0ff3018,0xaf2830d0,0xa6552df3,0x2f28aa41,
0x2655adf3,0xaf282a41,0xb0ff3018,0xaf2830d0,0x2df3b018,0x8000aa41,0xb0182df3,0xb10f30d0,0x3018adf3,0x310fb0d0,0x2df3b018,0x8000aa41,
0x2c98aa41,0x2f28adf3,0x3082b0d0,0x2f28b018,0xb08230d0,0xaf283018,0x2c98aa41,0x2f28adf3,0xb10f310f,0xb10f310f,0x0,0x0,
0x80008000,0x80008000,0xb10f310f,0xb10f310f,0x2c98aa41,0x2f28adf3,0xb08230d0,0xaf283018,0x3082b0d0,0x2f28b018,0x2c98aa41,0x2f28adf3,
0x2df3b018,0xaa41,0x3018adf3,0x310fb0d0,0xb0182df3,0xb10f30d0,0x2df3b018,0xaa41,0xb0ff3018,0xaf2830d0,0x2655adf3,0xaf282a41,
0xa6552df3,0x2f28aa41,0xb0ff3018,0xaf2830d0,0x2a41b018,0xb0182a41,0xb0d02df3,0xadf330d0,0x30d0adf3,0x2df3b0d0,0x2a41b018,0xb0182a41,
0x26552a41,0x3082adf3,0x30ffb0d0,0x2c98b018,0xb0ff30d0,0xac983018,0x26552a41,0x3082adf3,0xadf32a41,0xb0d03018,0xb01830d0,0xaa412df3,
0x3018b0d0,0x2a41adf3,0xadf32a41,0xb0d03018,0x3082b018,0x30ffb0d0,0x2c98adf3,0x2655aa41,0xac982df3,0xa6552a41,0x3082b018,0x30ffb0d0,
0xb10f310f,0xb10f310f,0x0,0x0,0x80008000,0x80008000,0xb10f310f,0xb10f310f,0x3082b018,0x30ffb0d0,0xac982df3,0xa6552a41,
0x2c98adf3,0x2655aa41,0x3082b018,0x30ffb0d0,0xadf32a41,0xb0d03018,0x3018b0d0,0x2a41adf3,0xb01830d0,0xaa412df3,0xadf32a41,0xb0d03018,
0x26552a41,0x3082adf3,0xb0ff30d0,0xac983018,0x30ffb0d0,0x2c98b018,0x26552a41,0x3082adf3,0xb018310f,0x2a412a41,0x2df30000,0x30d0b0d0,
0xadf38000,0xb0d030d0,0xb018310f,0x2a412a41,0x3082b10f,0x2655adf3,0xac980000,0xb0ff3018,0x2c988000,0x30ffb018,0x3082b10f,0x2655adf3,
0xb0d0310f,0xadf33018,0x2a410000,0x3018adf3,0xaa418000,0xb0182df3,0xb0d0310f,0xadf33018,0x30ffb10f,0x3082b0d0,0xa6550000,0xac982a41,
0x26558000,0x2c98aa41,0x30ffb10f,0x3082b0d0,0xb10f310f,0xb10f310f,0x0,0x0,0x80008000,0x80008000,0xb10f310f,0xb10f310f,
0x30ffb10f,0x3082b0d0,0x26550000,0x2c98aa41,0xa6558000,0xac982a41,0x30ffb10f,0x3082b0d0,0xb0d0310f,0xadf33018,0xaa410000,0xb0182df3,
0x2a418000,0x3018adf3,0xb0d0310f,0xadf33018,0x3082b10f,0x2655adf3,0x2c980000,0x30ffb018,0xac988000,0xb0ff3018,0x3082b10f,0x2655adf3,
0x310fb018,0x2a41b018,0xadf3,0xb0d02df3,0x80002df3,0x30d0adf3,0x310fb018,0x2a41b018,0xaf282a41,0xb0ff30d0,0xaf2830d0,0x26552a41,
0x2f28b0d0,0xa655aa41,0xaf282a41,0xb0ff30d0,0x2a41,0x2df3aa41,0x310fb0d0,0x3018b0d0,0xb10f30d0,0xb01830d0,0x2a41,0x2df3aa41,
0x2f28b018,0x2c98adf3,0xaf282df3,0xb0823018,0x2f28adf3,0x3082b018,0x2f28b018,0x2c98adf3,0xb10f310f,0xb10f310f,0x0,0x0,
0x80008000,0x80008000,0xb10f310f,0xb10f310f,0x2f28b018,0x2c98adf3,0x2f28adf3,0x3082b018,0xaf282df3,0xb0823018,0x2f28b018,0x2c98adf3,
0x80002a41,0x2df3aa41,0xb10f30d0,0xb01830d0,0x310fb0d0,0x3018b0d0,0x80002a41,0x2df3aa41,0xaf282a41,0xb0ff30d0,0x2f28b0d0,0xa655aa41,
0xaf2830d0,0x26552a41,0xaf282a41,0xb0ff30d0,0xb0182a41,0xb018310f,0xadf330d0,0x2df30000,0x2df3b0d0,0xadf38000,0xb0182a41,0xb018310f,
0xac983018,0x2c988000,0x3082adf3,0x3082b10f,0xb0822df3,0xb082310f,0xac983018,0x2c988000,0x30d0b018,0x30d0b10f,0x2a41adf3,0xaa410000,
0xaa412df3,0x2a418000,0x30d0b018,0x30d0b10f,0x2655aa41,0xa6550000,0xb0ff30d0,0xb0ff310f,0x30ffb0d0,0x30ffb10f,0x2655aa41,0xa6550000,
0xb10f310f,0xb10f310f,0x0,0x0,0x80008000,0x80008000,0xb10f310f,0xb10f310f,0x2655aa41,0xa6558000,0x30ffb0d0,0x30ffb10f,
0xb0ff30d0,0xb0ff310f,0x2655aa41,0xa6558000,0x30d0b018,0x30d0b10f,0xaa412df3,0x2a410000,0x2a41adf3,0xaa418000,0x30d0b018,0x30d0b10f,
0xac983018,0x2c980000,0xb0822df3,0xb082310f,0x3082adf3,0x3082b10f,0xac983018,0x2c980000,0x0,0x0,0x0,0x0,
0x0,0x0,0x0,0x0,0x0,0x0,0x0,0x0,0x0,0x0,0x0,0x0,
0x0,0x0,0x0,0x0,0x0,0x0,0x0,0x0,0x0,0x0,0x0,0x0,
0x0,0x0,0x0,0x0,0x0,0x0,0x0,0x0,0x0,0x0,0x0,0x0,
0x0,0x0,0x0,0x0,0x0,0x0,0x0,0x0,0x0,0x0,0x0,0x0,
0x0,0x0,0x0,0x0,0x0,0x0,0x0,0x0,0x0,0x0,0x0,0x0,
0x0,0x0,0x0,0x0,0x0,0x0,0x0,0x0,0x0,0x0,0x0,0x0,
0x0,0x0,0x0,0x0,0x0,0x0,0x0,0x0,0x0,0x0,0x0,0x0,
0x0,0x0,0x0,0x0,0x0,0x0,0x0,0x0,0x0,0x0,0x0,0x0,
0x0,0x0,0x0,0x0,0x0,0x0,0x0,0x0,0x0,0x0,0x0,0x0,
0x0,0x0,0x0,0x0,0x0,0x0,0x0,0x0,0x0,0x0,0x0,0x0,
0x0,0x0,0x0,0x0,0x2a412a41,0x310fb018,0x30d0b0d0,0xadf3,0xb0d030d0,0x80002df3,0x2a412a41,0x310fb018,
0x30ffb018,0x2f28b0d0,0x2655adf3,0xaf282a41,0xa6552df3,0x2f28aa41,0x30ffb018,0x2f28b0d0,0x2df3b018,0x8000aa41,0xb0182df3,0xb10f30d0,
0x3018adf3,0x310fb0d0,0x2df3b018,0x8000aa41,0xac982a41,0xaf282df3,0xb08230d0,0xaf283018,0x3082b0d0,0x2f28b018,0xac982a41,0xaf282df3,
0xb10f310f,0xb10f310f,0x0,0x0,0x80008000,0x80008000,0xb10f310f,0xb10f310f,0xac982a41,0xaf282df3,0x3082b0d0,0x2f28b018,
0xb08230d0,0xaf283018,0xac982a41,0xaf282df3,0x2df3b018,0xaa41,0x3018adf3,0x310fb0d0,0xb0182df3,0xb10f30d0,0x2df3b018,0xaa41,
0x30ffb018,0x2f28b0d0,0xa6552df3,0x2f28aa41,0x2655adf3,0xaf282a41,0x30ffb018,0x2f28b0d0,0x2a41b018,0xb0182a41,0xb0d02df3,0xadf330d0,
0x30d0adf3,0x2df3b0d0,0x2a41b018,0xb0182a41,0xa655aa41,0xb0822df3,0xb0ff30d0,0xac983018,0x30ffb0d0,0x2c98b018,0xa655aa41,0xb0822df3,
0xadf32a41,0xb0d03018,0xb01830d0,0xaa412df3,0x3018b0d0,0x2a41adf3,0xadf32a41,0xb0d03018,0xb0823018,0xb0ff30d0,0xac982df3,0xa6552a41,
0x2c98adf3,0x2655aa41,0xb0823018,0xb0ff30d0,0xb10f310f,0xb10f310f,0x0,0x0,0x80008000,0x80008000,0xb10f310f,0xb10f310f,
0xb0823018,0xb0ff30d0,0x2c98adf3,0x2655aa41,0xac982df3,0xa6552a41,0xb0823018,0xb0ff30d0,0xadf32a41,0xb0d03018,0x3018b0d0,0x2a41adf3,
0xb01830d0,0xaa412df3,0xadf32a41,0xb0d03018,0xa655aa41,0xb0822df3,0x30ffb0d0,0x2c98b018,0xb0ff30d0,0xac983018,0xa655aa41,0xb0822df3,
0xb018310f,0x2a412a41,0xadf30000,0xb0d030d0,0x2df38000,0x30d0b0d0,0xb018310f,0x2a412a41,0xaf28310f,0x2f280000,0xaf280000,0xaf28310f,
0x2f288000,0x2f28b10f,0xaf28310f,0x2f280000,0xadf3310f,0x30d0aa41,0xb0180000,0xaa4130d0,0x30188000,0x2a41b0d0,0xadf3310f,0x30d0aa41,
0xac98310f,0x30ffadf3,0xb0820000,0x26553018,0x30828000,0xa655b018,0xac98310f,0x30ffadf3,0xaa41310f,0x3018b018,0xb0d00000,0x2df32df3,
0x30d08000,0xadf3adf3,0xaa41310f,0x3018b018,0xa655310f,0x2c98b0d0,0xb0ff0000,0x30822a41,0x30ff8000,0xb082aa41,0xa655310f,0x2c98b0d0,
0x8000310f,0xb10f,0xb10f0000,0x310f0000,0x310f8000,0xb10f8000,0x8000310f,0xb10f,0x2655310f,0xac98b0d0,0xb0ff0000,0x3082aa41,
0x30ff8000,0xb0822a41,0x2655310f,0xac98b0d0,0x310fb018,0x2a41b018,0x2df3,0x30d0adf3,0x8000adf3,0xb0d02df3,0x310fb018,0x2a41b018,
0x2f28b10f,0xaf288000,0x2f280000,0x2f28b10f,0xaf288000,0xaf28310f,0x2f28b10f,0xaf288000,0xb018,0xb0d03018,0x310fadf3,0xaa41adf3,
0xb10f2df3,0x2a412df3,0xb018,0xb0d03018,0xaf28aa41,0xa65530d0,0x2f28b0d0,0xb0ff2a41,0xaf2830d0,0x30ffaa41,0xaf28aa41,0xa65530d0,
0xb10f2a41,0x30182a41,0xb0d0,0xadf330d0,0x800030d0,0x2df3b0d0,0xb10f2a41,0x30182a41,0xaf283018,0x3082adf3,0xaf28adf3,0x2c983018,
0x2f282df3,0xac98b018,0xaf283018,0x3082adf3,0x8000310f,0xb10f,0xb10f0000,0x310f0000,0x310f8000,0xb10f8000,0x8000310f,0xb10f,
0x2f283018,0xb082adf3,0xaf282df3,0x2c98b018,0x2f28adf3,0xac983018,0x2f283018,0xb082adf3,0xb0182a41,0xb018310f,0x2df3b0d0,0xadf30000,
0xadf330d0,0x2df38000,0xb0182a41,0xb018310f,0xaf28310f,0x2f280000,0xaf280000,0xaf28310f,0x2f288000,0x2f28b10f,0xaf28310f,0x2f280000,
0x2df32a41,0x2df3b10f,0xb01830d0,0x30180000,0x3018b0d0,0xb0188000,0x2df32a41,0x2df3b10f,0x3082b018,0xb0828000,0x2c982df3,0x2c98b10f,
0xac98adf3,0xac98310f,0x3082b018,0xb0828000,0xaa41b018,0xaa41310f,0x30d0adf3,0xb0d00000,0xb0d02df3,0x30d08000,0xaa41b018,0xaa41310f,
0xb0ff2a41,0x30ff0000,0xa655b0d0,0xa655310f,0x265530d0,0x2655b10f,0xb0ff2a41,0x30ff0000,0x8000310f,0xb10f,0xb10f0000,0x310f0000,
0x310f8000,0xb10f8000,0x8000310f,0xb10f,0x30ff2a41,0xb0ff8000,0xa65530d0,0xa655b10f,0x2655b0d0,0x2655310f,0x30ff2a41,0xb0ff8000,
0x2a412a41,0x310fb018,0xb0d030d0,0x2df3,0x30d0b0d0,0x8000adf3,0x2a412a41,0x310fb018,0x2f28b10f,0xaf288000,0x2f280000,0x2f28b10f,
0xaf288000,0xaf28310f,0x2f28b10f,0xaf288000,0xb0d02a41,0x80003018,0x2a41b0d0,0xb10f2df3,0xaa4130d0,0x310fadf3,0xb0d02a41,0x80003018,
0x26553018,0x2f28b0d0,0xb0ff2df3,0x2f282a41,0x30ffadf3,0xaf28aa41,0x26553018,0x2f28b0d0,0x3018b018,0xb10f2a41,0x2df32df3,0xb0d0,
0xadf3adf3,0x800030d0,0x3018b018,0xb10f2a41,0xb082aa41,0x2f282df3,0x2c98b0d0,0xaf283018,0xac9830d0,0x2f28b018,0xb082aa41,0x2f282df3,
0x8000310f,0xb10f,0xb10f0000,0x310f0000,0x310f8000,0xb10f8000,0x8000310f,0xb10f,0x3082aa41,0xaf282df3,0x2c9830d0,0xaf28b018,
0xac98b0d0,0x2f283018,0x3082aa41,0xaf282df3,0x2a41b018,0xb0182a41,0x30d0adf3,0x2df3b0d0,0xb0d02df3,0xadf330d0,0x2a41b018,0xb0182a41,
0xaf28310f,0x2f280000,0xaf280000,0xaf28310f,0x2f288000,0x2f28b10f,0xaf28310f,0x2f280000,0x30d0b018,0xadf3aa41,0x2a412df3,0x3018b0d0,
0xaa41adf3,0xb01830d0,0x30d0b018,0xadf3aa41,0xb0ff2a41,0x2c982df3,0x2655b0d0,0xb0823018,0xa65530d0,0x3082b018,0xb0ff2a41,0x2c982df3,
0x30182a41,0xaa41b018,0xadf330d0,0x30d0adf3,0x2df3b0d0,0xb0d02df3,0x30182a41,0xaa41b018,0xac98b018,0x265530d0,0x3082adf3,0xb0ff2a41,
0xb0822df3,0x30ffaa41,0xac98b018,0x265530d0,0x8000310f,0xb10f,0xb10f0000,0x310f0000,0x310f8000,0xb10f8000,0x8000310f,0xb10f,
0x2c98b018,0xa65530d0,0x30822df3,0xb0ffaa41,0xb082adf3,0x30ff2a41,0x2c98b018,0xa65530d0,0xb018310f,0x2a412a41,0xadf30000,0xb0d030d0,
0x2df38000,0x30d0b0d0,0xb018310f,0x2a412a41,0x2f28b10f,0xaf288000,0x2f280000,0x2f28b10f,0xaf288000,0xaf28310f,0x2f28b10f,0xaf288000,
0xadf3310f,0x30d0aa41,0xb0180000,0xaa4130d0,0x30188000,0x2a41b0d0,0xadf3310f,0x30d0aa41,0x2c98b10f,0xb0ff2df3,0x30820000,0xa655b018,
0xb0828000,0x26553018,0x2c98b10f,0xb0ff2df3,0xaa41310f,0x3018b018,0xb0d00000,0x2df32df3,0x30d08000,0xadf3adf3,0xaa41310f,0x3018b018,
0x2655b10f,0xac9830d0,0x30ff0000,0xb082aa41,0xb0ff8000,0x30822a41,0x2655b10f,0xac9830d0,0x8000310f,0xb10f,0xb10f0000,0x310f0000,
0x310f8000,0xb10f8000,0x8000310f,0xb10f,0xa655b10f,0x2c9830d0,0x30ff0000,0xb0822a41,0xb0ff8000,0x3082aa41,0xa655b10f,0x2c9830d0,
0x310fb018,0x2a41b018,0x2df3,0x30d0adf3,0x8000adf3,0xb0d02df3,0x310fb018,0x2a41b018,0xaf28310f,0x2f280000,0xaf280000,0xaf28310f,
0x2f288000,0x2f28b10f,0xaf28310f,0x2f280000,0xb018,0xb0d03018,0x310fadf3,0xaa41adf3,0xb10f2df3,0x2a412df3,0xb018,0xb0d03018,
0x2f282a41,0x2655b0d0,0xaf2830d0,0x30ffaa41,0x2f28b0d0,0xb0ff2a41,0x2f282a41,0x2655b0d0,0xb10f2a41,0x30182a41,0xb0d0,0xadf330d0,
0x800030d0,0x2df3b0d0,0xb10f2a41,0x30182a41,0x2f28b018,0xb0822df3,0x2f282df3,0xac98b018,0xaf28adf3,0x2c983018,0x2f28b018,0xb0822df3,
0x8000310f,0xb10f,0xb10f0000,0x310f0000,0x310f8000,0xb10f8000,0x8000310f,0xb10f,0xaf28b018,0x30822df3,0x2f28adf3,0xac983018,
0xaf282df3,0x2c98b018,0xaf28b018,0x30822df3,0xb0182a41,0xb018310f,0x2df3b0d0,0xadf30000,0xadf330d0,0x2df38000,0xb0182a41,0xb018310f,
0x2f28b10f,0xaf288000,0x2f280000,0x2f28b10f,0xaf288000,0xaf28310f,0x2f28b10f,0xaf288000,0x2df32a41,0x2df3b10f,0xb01830d0,0x30180000,
0x3018b0d0,0xb0188000,0x2df32a41,0x2df3b10f,0xb0823018,0x30820000,0xac98adf3,0xac98310f,0x2c982df3,0x2c98b10f,0xb0823018,0x30820000,
0xaa41b018,0xaa41310f,0x30d0adf3,0xb0d00000,0xb0d02df3,0x30d08000,0xaa41b018,0xaa41310f,0x30ffaa41,0xb0ff8000,0x265530d0,0x2655b10f,
0xa655b0d0,0xa655310f,0x30ffaa41,0xb0ff8000,0x8000310f,0xb10f,0xb10f0000,0x310f0000,0x310f8000,0xb10f8000,0x8000310f,0xb10f,
0xb0ffaa41,0x30ff0000,0x2655b0d0,0x2655310f,0xa65530d0,0xa655b10f,0xb0ffaa41,0x30ff0000,0x0,0x0,0x0,0x0,
0x0,0x0,0x0,0x0,0x0,0x0,0x0,0x0,0x0,0x0,0x0,0x0,
0x0,0x0,0x0,0x0,0x0,0x0,0x0,0x0,0x0,0x0,0x0,0x0,
0x0,0x0,0x0,0x0,0x0,0x0,0x0,0x0,0x0,0x0,0x0,0x0,
0x0,0x0,0x0,0x0,0x0,0x0,0x0,0x0,0x0,0x0,0x0,0x0,
0x0,0x0,0x0,0x0,0x0,0x0,0x0,0x0,0x0,0x0,0x0,0x0,
0x0,0x0,0x0,0x0,0x0,0x0,0x0,0x0,0x0,0x0,0x0,0x0,
0x0,0x0,0x0,0x0,0x0,0x0,0x0,0x0,0x0,0x0,0x0,0x0,
0x0,0x0,0x0,0x0,0x0,0x0,0x0,0x0,0x0,0x0,0x0,0x0,
0x0,0x0,0x0,0x0,0x0,0x0,0x0,0x0,0x0,0x0,0x0,0x0,
0x0,0x0,0x0,0x0,0x0,0x0,0x0,0x0,0x0,0x0,0x0,0x0,
0x0,0x0,0x0,0x0,0x2a412a41,0x310fb018,0xb0d030d0,0x2df3,0x30d0b0d0,0x8000adf3,0x2a412a41,0x310fb018,
0xaf28310f,0x2f280000,0xaf280000,0xaf28310f,0x2f288000,0x2f28b10f,0xaf28310f,0x2f280000,0xb0d02a41,0x80003018,0x2a41b0d0,0xb10f2df3,
0xaa4130d0,0x310fadf3,0xb0d02a41,0x80003018,0xa655b018,0xaf2830d0,0x30ffadf3,0xaf28aa41,0xb0ff2df3,0x2f282a41,0xa655b018,0xaf2830d0,
0x3018b018,0xb10f2a41,0x2df32df3,0xb0d0,0xadf3adf3,0x800030d0,0x3018b018,0xb10f2a41,0x30822a41,0xaf28adf3,0xac9830d0,0x2f28b018,
0x2c98b0d0,0xaf283018,0x30822a41,0xaf28adf3,0x8000310f,0xb10f,0xb10f0000,0x310f0000,0x310f8000,0xb10f8000,0x8000310f,0xb10f,
0xb0822a41,0x2f28adf3,0xac98b0d0,0x2f283018,0x2c9830d0,0xaf28b018,0xb0822a41,0x2f28adf3,0x2a41b018,0xb0182a41,0x30d0adf3,0x2df3b0d0,
0xb0d02df3,0xadf330d0,0x2a41b018,0xb0182a41,0x2f28b10f,0xaf288000,0x2f280000,0x2f28b10f,0xaf288000,0xaf28310f,0x2f28b10f,0xaf288000,
0x30d0b018,0xadf3aa41,0x2a412df3,0x3018b0d0,0xaa41adf3,0xb01830d0,0x30d0b018,0xadf3aa41,0x30ffaa41,0xac98adf3,0xa65530d0,0x3082b018,
0x2655b0d0,0xb0823018,0x30ffaa41,0xac98adf3,0x30182a41,0xaa41b018,0xadf330d0,0x30d0adf3,0x2df3b0d0,0xb0d02df3,0x30182a41,0xaa41b018,
0x2c983018,0xa655b0d0,0xb0822df3,0x30ffaa41,0x3082adf3,0xb0ff2a41,0x2c983018,0xa655b0d0,0x8000310f,0xb10f,0xb10f0000,0x310f0000,
0x310f8000,0xb10f8000,0x8000310f,0xb10f,0xac983018,0x2655b0d0,0xb082adf3,0x30ff2a41,0x30822df3,0xb0ffaa41,0xac983018,0x2655b0d0,
0x2a41310f,0xb018b018,0xb0d00000,0x2df3adf3,0x30d08000,0xadf32df3,0x2a41310f,0xb018b018,0x2c98310f,0xb0ffadf3,0xb0820000,0x2655b018,
0x30828000,0xa6553018,0x2c98310f,0xb0ffadf3,0x2df3310f,0xb0d0aa41,0xb0180000,0xaa41b0d0,0x30188000,0x2a4130d0,0x2df3310f,0xb0d0aa41,
0x2f28310f,0xaf288000,0xaf280000,0xaf28b10f,0x2f288000,0x2f28310f,0x2f28310f,0xaf288000,0x3018310f,0xaa412a41,0xadf30000,0xb0d0b0d0,
0x2df38000,0x30d030d0,0x3018310f,0xaa412a41,0x3082310f,0x26552df3,0xac980000,0xb0ffb018,0x2c988000,0x30ff3018,0x3082310f,0x26552df3,
0x30d0310f,0x2df33018,0xaa410000,0xb018adf3,0x2a418000,0x30182df3,0x30d0310f,0x2df33018,0x30ff310f,0x308230d0,0xa6550000,0xac98aa41,
0x26558000,0x2c982a41,0x30ff310f,0x308230d0,0x310f2a41,0xb0182a41,0x30d0,0xadf3b0d0,0x8000b0d0,0x2df330d0,0x310f2a41,0xb0182a41,
0x2f28aa41,0x265530d0,0x2f2830d0,0xb0ffaa41,0xaf28b0d0,0x30ff2a41,0x2f28aa41,0x265530d0,0xb018,0x30d03018,0x310f2df3,0xaa412df3,
0xb10fadf3,0x2a41adf3,0xb018,0x30d03018,0xaf28b10f,0x2f280000,0x2f280000,0x2f28310f,0xaf288000,0xaf28b10f,0xaf28b10f,0x2f280000,
0xb10fb018,0xaa41b018,0xadf3,0x30d02df3,0x80002df3,0xb0d0adf3,0xb10fb018,0xaa41b018,0xaf28aa41,0xb0ffb0d0,0xaf28b0d0,0x2655aa41,
0x2f2830d0,0xa6552a41,0xaf28aa41,0xb0ffb0d0,0x80002a41,0xadf3aa41,0xb10fb0d0,0xb018b0d0,0x310f30d0,0x301830d0,0x80002a41,0xadf3aa41,
0x2f283018,0x2c982df3,0xaf28adf3,0xb082b018,0x2f282df3,0x30823018,0x2f283018,0x2c982df3,0x2a41b018,0x2a41310f,0x30d02df3,0xb0d00000,
0xb0d0adf3,0x30d08000,0x2a41b018,0x2a41310f,0xb082b018,0x30820000,0x2c98adf3,0x2c98310f,0xac982df3,0xac98b10f,0xb082b018,0x30820000,
0xadf32a41,0xadf3b10f,0xb018b0d0,0x30180000,0x301830d0,0xb0188000,0xadf32a41,0xadf3b10f,0x2f28310f,0xaf288000,0xaf280000,0xaf28b10f,
0x2f288000,0x2f28310f,0x2f28310f,0xaf288000,0x30182a41,0x3018310f,0x2df330d0,0xadf30000,0xadf3b0d0,0x2df38000,0x30182a41,0x3018310f,
0xac98b018,0x2c980000,0x30822df3,0x3082310f,0xb082adf3,0xb082b10f,0xac98b018,0x2c980000,0xb0d0b018,0xb0d0b10f,0xaa41adf3,0x2a410000,
0x2a412df3,0xaa418000,0xb0d0b018,0xb0d0b10f,0x26552a41,0xa6558000,0xb0ffb0d0,0xb0ffb10f,0x30ff30d0,0x30ff310f,0x26552a41,0xa6558000,
0xb018b018,0x310f2a41,0x2df3adf3,0x30d0,0xadf32df3,0x8000b0d0,0xb018b018,0x310f2a41,0xa6553018,0xaf28b0d0,0xb0ffadf3,0x2f28aa41,
0x30ff2df3,0xaf282a41,0xa6553018,0xaf28b0d0,0x30d02a41,0x80003018,0x2a4130d0,0xb10fadf3,0xaa41b0d0,0x310f2df3,0x30d02a41,0x80003018,
0xaf28b10f,0x2f280000,0x2f280000,0x2f28310f,0xaf288000,0xaf28b10f,0xaf28b10f,0x2f280000,0xaa412a41,0xb10fb018,0xb0d0b0d0,0xadf3,
0x30d030d0,0x80002df3,0xaa412a41,0xb10fb018,0x30ff3018,0x2f2830d0,0x26552df3,0xaf28aa41,0xa655adf3,0x2f282a41,0x30ff3018,0x2f2830d0,
0xadf3b018,0xaa41,0x30182df3,0x310f30d0,0xb018adf3,0xb10fb0d0,0xadf3b018,0xaa41,0xac98aa41,0xaf28adf3,0xb082b0d0,0xaf28b018,
0x308230d0,0x2f283018,0xac98aa41,0xaf28adf3,0xb0182a41,0x2a41b018,0xadf3b0d0,0x30d02df3,0x2df330d0,0xb0d0adf3,0xb0182a41,0x2a41b018,
0x30ff2a41,0xac982df3,0x265530d0,0xb082b018,0xa655b0d0,0x30823018,0x30ff2a41,0xac982df3,0xb0d0b018,0x2df3aa41,0x2a41adf3,0x301830d0,
0xaa412df3,0xb018b0d0,0xb0d0b018,0x2df3aa41,0x2f28310f,0xaf288000,0xaf280000,0xaf28b10f,0x2f288000,0x2f28310f,0x2f28310f,0xaf288000,
0xaa41b018,0x30182a41,0x30d02df3,0x2df330d0,0xb0d0adf3,0xadf3b0d0,0xaa41b018,0x30182a41,0xa6552a41,0xb082adf3,0xb0ffb0d0,0xac98b018,
0x30ff30d0,0x2c983018,0xa6552a41,0xb082adf3,0x2df32a41,0x30d03018,0x301830d0,0x2a412df3,0xb018b0d0,0xaa41adf3,0x2df32a41,0x30d03018,
0xb082b018,0xb0ffb0d0,0xac98adf3,0xa655aa41,0x2c982df3,0x26552a41,0xb082b018,0xb0ffb0d0,0x2a41310f,0xb018b018,0xb0d00000,0x2df3adf3,
0x30d08000,0xadf32df3,0x2a41310f,0xb018b018,0xac98b10f,0x30ff2df3,0x30820000,0xa6553018,0xb0828000,0x2655b018,0xac98b10f,0x30ff2df3,
0x2df3310f,0xb0d0aa41,0xb0180000,0xaa41b0d0,0x30188000,0x2a4130d0,0x2df3310f,0xb0d0aa41,0xaf28b10f,0x2f280000,0x2f280000,0x2f28310f,
0xaf288000,0xaf28b10f,0xaf28b10f,0x2f280000,0x3018310f,0xaa412a41,0xadf30000,0xb0d0b0d0,0x2df38000,0x30d030d0,0x3018310f,0xaa412a41,
0xb082b10f,0xa655adf3,0x2c980000,0x30ff3018,0xac988000,0xb0ffb018,0xb082b10f,0xa655adf3,0x30d0310f,0x2df33018,0xaa410000,0xb018adf3,
0x2a418000,0x30182df3,0x30d0310f,0x2df33018,0xb0ffb10f,0xb082b0d0,0x26550000,0x2c982a41,0xa6558000,0xac98aa41,0xb0ffb10f,0xb082b0d0,
0x310f2a41,0xb0182a41,0x30d0,0xadf3b0d0,0x8000b0d0,0x2df330d0,0x310f2a41,0xb0182a41,0xaf282a41,0xa655b0d0,0xaf28b0d0,0x30ff2a41,
0x2f2830d0,0xb0ffaa41,0xaf282a41,0xa655b0d0,0xb018,0x30d03018,0x310f2df3,0xaa412df3,0xb10fadf3,0x2a41adf3,0xb018,0x30d03018,
0x2f28310f,0xaf288000,0xaf280000,0xaf28b10f,0x2f288000,0x2f28310f,0x2f28310f,0xaf288000,0xb10fb018,0xaa41b018,0xadf3,0x30d02df3,
0x80002df3,0xb0d0adf3,0xb10fb018,0xaa41b018,0x2f282a41,0x30ff30d0,0x2f2830d0,0xa6552a41,0xaf28b0d0,0x2655aa41,0x2f282a41,0x30ff30d0,
0x80002a41,0xadf3aa41,0xb10fb0d0,0xb018b0d0,0x310f30d0,0x301830d0,0x80002a41,0xadf3aa41,0xaf28b018,0xac98adf3,0x2f282df3,0x30823018,
0xaf28adf3,0xb082b018,0xaf28b018,0xac98adf3,0x2a41b018,0x2a41310f,0x30d02df3,0xb0d00000,0xb0d0adf3,0x30d08000,0x2a41b018,0x2a41310f,
0x30823018,0xb0828000,0xac982df3,0xac98b10f,0x2c98adf3,0x2c98310f,0x30823018,0xb0828000,0xadf32a41,0xadf3b10f,0xb018b0d0,0x30180000,
0x301830d0,0xb0188000,0xadf32a41,0xadf3b10f,0xaf28b10f,0x2f280000,0x2f280000,0x2f28310f,0xaf288000,0xaf28b10f,0xaf28b10f,0x2f280000,
0x30182a41,0x3018310f,0x2df330d0,0xadf30000,0xadf3b0d0,0x2df38000,0x30182a41,0x3018310f,0x2c983018,0xac988000,0xb082adf3,0xb082b10f,
0x30822df3,0x3082310f,0x2c983018,0xac988000,0xb0d0b018,0xb0d0b10f,0xaa41adf3,0x2a410000,0x2a412df3,0xaa418000,0xb0d0b018,0xb0d0b10f,
0xa655aa41,0x26550000,0x30ff30d0,0x30ff310f,0xb0ffb0d0,0xb0ffb10f,0xa655aa41,0x26550000,0x0,0x0,0x0,0x0,
0x0,0x0,0x0,0x0,0x0,0x0,0x0,0x0,0x0,0x0,0x0,0x0,
0x0,0x0,0x0,0x0,0x0,0x0,0x0,0x0,0x0,0x0,0x0,0x0,
0x0,0x0,0x0,0x0,0x0,0x0,0x0,0x0,0x0,0x0,0x0,0x0,
0x0,0x0,0x0,0x0,0x0,0x0,0x0,0x0,0x0,0x0,0x0,0x0,
0x0,0x0,0x0,0x0,0x0,0x0,0x0,0x0,0x0,0x0,0x0,0x0,
0x0,0x0,0x0,0x0,0x0,0x0,0x0,0x0,0x0,0x0,0x0,0x0,
0x0,0x0,0x0,0x0,0x0,0x0,0x0,0x0,0x0,0x0,0x0,0x0,
0x0,0x0,0x0,0x0,0x0,0x0,0x0,0x0,0x0,0x0,0x0,0x0,
0x0,0x0,0x0,0x0,0x0,0x0,0x0,0x0,0x0,0x0,0x0,0x0,
0x0,0x0,0x0,0x0,0x0,0x0,0x0,0x0,0x0,0x0,0x0,0x0,
0x0,0x0,0x0,0x0,0xb018b018,0x310f2a41,0x2df3adf3,0x30d0,0xadf32df3,0x8000b0d0,0xb018b018,0x310f2a41,
0x2655b018,0x2f2830d0,0x30ff2df3,0xaf282a41,0xb0ffadf3,0x2f28aa41,0x2655b018,0x2f2830d0,0x30d02a41,0x80003018,0x2a4130d0,0xb10fadf3,
0xaa41b0d0,0x310f2df3,0x30d02a41,0x80003018,0x2f28310f,0xaf288000,0xaf280000,0xaf28b10f,0x2f288000,0x2f28310f,0x2f28310f,0xaf288000,
0xaa412a41,0xb10fb018,0xb0d0b0d0,0xadf3,0x30d030d0,0x80002df3,0xaa412a41,0xb10fb018,0xb0ffb018,0xaf28b0d0,0xa655adf3,0x2f282a41,
0x26552df3,0xaf28aa41,0xb0ffb018,0xaf28b0d0,0xadf3b018,0xaa41,0x30182df3,0x310f30d0,0xb018adf3,0xb10fb0d0,0xadf3b018,0xaa41,
0x2c982a41,0x2f282df3,0x308230d0,0x2f283018,0xb082b0d0,0xaf28b018,0x2c982a41,0x2f282df3,0xb0182a41,0x2a41b018,0xadf3b0d0,0x30d02df3,
0x2df330d0,0xb0d0adf3,0xb0182a41,0x2a41b018,0xb0ffaa41,0x2c98adf3,0xa655b0d0,0x30823018,0x265530d0,0xb082b018,0xb0ffaa41,0x2c98adf3,
0xb0d0b018,0x2df3aa41,0x2a41adf3,0x301830d0,0xaa412df3,0xb018b0d0,0xb0d0b018,0x2df3aa41,0xaf28b10f,0x2f280000,0x2f280000,0x2f28310f,
0xaf288000,0xaf28b10f,0xaf28b10f,0x2f280000,0xaa41b018,0x30182a41,0x30d02df3,0x2df330d0,0xb0d0adf3,0xadf3b0d0,0xaa41b018,0x30182a41,
0x2655aa41,0x30822df3,0x30ff30d0,0x2c983018,0xb0ffb0d0,0xac98b018,0x2655aa41,0x30822df3,0x2df32a41,0x30d03018,0x301830d0,0x2a412df3,
0xb018b0d0,0xaa41adf3,0x2df32a41,0x30d03018,0x30823018,0x30ff30d0,0x2c982df3,0x26552a41,0xac98adf3,0xa655aa41,0x30823018,0x30ff30d0,
};
__device__ __attribute__((aligned(16))) const unsigned int CB2_TAB[4608] = {
0x8000310f,0x8000310f,0x8000310f,0x8000310f,0x8000310f,0xa65530ff,0xaa4130d0,0xac983082,0x8000310f,0xaa4130d0,0xadf33018,0xb0182df3,
0x8000310f,0xac983082,0xb0182df3,0xb0ff2655,0x8000310f,0xadf33018,0xb0d02a41,0xb0d0aa41,0x8000310f,0xaf282f28,0xb10f0000,0xaf28af28,
0x8000310f,0xb0182df3,0xb0d0aa41,0xaa41b0d0,0x8000310f,0xb0822c98,0xb018adf3,0x2655b0ff,0x8000310f,0xb0d02a41,0xadf3b018,0x2df3b018,
0x8000310f,0xb0ff2655,0xaa41b0d0,0x3082ac98,0x8000310f,0xb10f0000,0x8000b10f,0x310f8000,0x8000310f,0xb0ffa655,0x2a41b0d0,0x30822c98,
0x8000310f,0xb0d0aa41,0x2df3b018,0x2df33018,0x8000310f,0xb082ac98,0x3018adf3,0x265530ff,0x8000310f,0xb018adf3,0x30d0aa41,0xaa4130d0,
0x8000310f,0xaf28af28,0x310f8000,0xaf282f28,0x8000310f,0x8000310f,0x8000310f,0x8000310f,0xadf33018,0xaf282f28,0xb0182df3,0xb0822c98,
0xb0d02a41,0xb10f0000,0xb0d0aa41,0xb018adf3,0xb0d0aa41,0xaf28af28,0xaa41b0d0,0x2655b0ff,0xadf3b018,0x8000b10f,0x2df3b018,0x30d0aa41,
0x8000b10f,0x2f28af28,0x310f8000,0x2f282f28,0x2df3b018,0x310f8000,0x2df33018,0xaa4130d0,0x30d0aa41,0x2f282f28,0xaa4130d0,0xb0ff2655,
0x30d02a41,0x8000310f,0xb0d02a41,0xadf3b018,0x2df33018,0xaf282f28,0xb018adf3,0x2c98b082,0x8000310f,0xb10f0000,0x8000b10f,0x310f8000,
0xadf33018,0xaf28af28,0x3018adf3,0x2c983082,0xb0d02a41,0x8000b10f,0x30d02a41,0xadf33018,0xb0d0aa41,0x2f28af28,0x2a4130d0,0xb0ffa655,
0xadf3b018,0x310f8000,0xadf33018,0xaa41b0d0,0x8000b10f,0x2f282f28,0xb10f0000,0x2f28af28,0x8000310f,0x8000310f,0x8000310f,0x8000310f,
0xb0d02a41,0xb0ff2655,0xb10f0000,0xb0ffa655,0xadf3b018,0xaa41b0d0,0x8000b10f,0x2a41b0d0,0x2df3b018,0x3082ac98,0x310f8000,0x30822c98,
0x30d02a41,0x2df33018,0x8000310f,0xadf33018,0x8000310f,0xaf282f28,0xb10f0000,0xaf28af28,0xb0d02a41,0xb018adf3,0x8000b10f,0x3018adf3,
0xadf3b018,0x2c98b082,0x310f8000,0x2c983082,0x2df3b018,0x30d02a41,0x8000310f,0xb0d02a41,0x30d02a41,0xa65530ff,0xb10f0000,0xa655b0ff,
0x8000310f,0xb10f0000,0x8000b10f,0x310f8000,0xb0d02a41,0xa655b0ff,0x310f8000,0xa65530ff,0xadf3b018,0x30d0aa41,0x8000310f,0xb0d0aa41,
0x2df3b018,0x2c983082,0xb10f0000,0x2c98b082,0x30d02a41,0xb0182df3,0x8000b10f,0x30182df3,0x8000310f,0xaf28af28,0x310f8000,0xaf282f28,
0x8000310f,0x8000310f,0x8000310f,0x8000310f,0xb0d0aa41,0xb082ac98,0xb018adf3,0xaf28af28,0x2df3b018,0x3018adf3,0x30d0aa41,0x310f8000,
0x2df33018,0x265530ff,0xaa4130d0,0xaf282f28,0xb0d02a41,0xb0d0aa41,0xadf3b018,0x8000b10f,0x8000b10f,0x2f28af28,0x310f8000,0x2f282f28,
0x30d02a41,0x2a4130d0,0xadf33018,0xb10f0000,0xadf33018,0xb0ffa655,0xaa41b0d0,0x2f28af28,0xadf3b018,0x2df3b018,0x30d02a41,0x8000310f,
0x30d0aa41,0x2c983082,0xb0182df3,0xaf28af28,0x8000310f,0xb10f0000,0x8000b10f,0x310f8000,0xb0d0aa41,0x2c98b082,0x30182df3,0xaf282f28,
0x2df3b018,0x2df33018,0xb0d02a41,0x8000b10f,0x2df33018,0xb0ff2655,0x2a41b0d0,0x2f282f28,0xb0d02a41,0x2a41b0d0,0x2df33018,0xb10f0000,
0x8000b10f,0x2f282f28,0xb10f0000,0x2f28af28,0x310f0000,0x310f0000,0x310f0000,0x310f0000,0x310f0000,0x30ff2655,0x30d02a41,0x30822c98,
0x310f0000,0x30d02a41,0x30182df3,0x2df33018,0x310f0000,0x30822c98,0x2df33018,0x265530ff,0x310f0000,0x30182df3,0x2a4130d0,0xaa4130d0,
0x310f0000,0x2f282f28,0x310f,0xaf282f28,0x310f0000,0x2df33018,0xaa4130d0,0xb0d02a41,0x310f0000,0x2c983082,0xadf33018,0xb0ffa655,
0x310f0000,0x2a4130d0,0xb0182df3,0xb018adf3,0x310f0000,0x265530ff,0xb0d02a41,0xac98b082,0x310f0000,0x310f,0xb10f0000,0x8000b10f,
0x310f0000,0xa65530ff,0xb0d0aa41,0x2c98b082,0x310f0000,0xaa4130d0,0xb018adf3,0x3018adf3,0x310f0000,0xac983082,0xadf3b018,0x30ffa655,
0x310f0000,0xadf33018,0xaa41b0d0,0x30d02a41,0x310f0000,0xaf282f28,0x8000b10f,0x2f282f28,0x310f0000,0x310f0000,0x310f0000,0x310f0000,
0x30182df3,0x2f282f28,0x2df33018,0x2c983082,0x2a4130d0,0x310f,0xaa4130d0,0xadf33018,0xaa4130d0,0xaf282f28,0xb0d02a41,0xb0ffa655,
0xb0182df3,0xb10f0000,0xb018adf3,0xaa41b0d0,0xb10f0000,0xaf28af28,0x8000b10f,0x2f28af28,0xb018adf3,0x8000b10f,0x3018adf3,0x30d02a41,
0xaa41b0d0,0x2f28af28,0x30d02a41,0x265530ff,0x2a41b0d0,0x310f0000,0x2a4130d0,0xb0182df3,0x3018adf3,0x2f282f28,0xadf33018,0xb082ac98,
0x310f0000,0x310f,0xb10f0000,0x8000b10f,0x30182df3,0xaf282f28,0xadf3b018,0x3082ac98,0x2a4130d0,0xb10f0000,0x2a41b0d0,0x30182df3,
0xaa4130d0,0xaf28af28,0x30d0aa41,0xa65530ff,0xb0182df3,0x8000b10f,0x30182df3,0xb0d02a41,0xb10f0000,0x2f28af28,0x310f,0xaf28af28,
0x310f0000,0x310f0000,0x310f0000,0x310f0000,0x2a4130d0,0x265530ff,0x310f,0xa65530ff,0xb0182df3,0xb0d02a41,0xb10f0000,0xb0d0aa41,
0xb018adf3,0xac98b082,0x8000b10f,0x2c98b082,0x2a41b0d0,0x3018adf3,0x310f0000,0x30182df3,0x310f0000,0x2f282f28,0x310f,0xaf282f28,
0x2a4130d0,0xadf33018,0xb10f0000,0xadf3b018,0xb0182df3,0xb082ac98,0x8000b10f,0x3082ac98,0xb018adf3,0x2a41b0d0,0x310f0000,0x2a4130d0,
0x2a41b0d0,0x30ff2655,0x310f,0xb0ff2655,0x310f0000,0x310f,0xb10f0000,0x8000b10f,0x2a4130d0,0xb0ff2655,0x8000b10f,0x30ff2655,
0xb0182df3,0xaa41b0d0,0x310f0000,0xaa4130d0,0xb018adf3,0x3082ac98,0x310f,0xb082ac98,0x2a41b0d0,0x2df33018,0xb10f0000,0x2df3b018,
0x310f0000,0xaf282f28,0x8000b10f,0x2f282f28,0x310f0000,0x310f0000,0x310f0000,0x310f0000,0xaa4130d0,0xac983082,0xadf33018,0xaf282f28,
0xb018adf3,0xadf3b018,0xaa41b0d0,0x8000b10f,0x3018adf3,0x30ffa655,0x30d02a41,0x2f282f28,0x2a4130d0,0xaa4130d0,0xb0182df3,0xb10f0000,
0xb10f0000,0xaf28af28,0x8000b10f,0x2f28af28,0x2a41b0d0,0x30d0aa41,0x30182df3,0x310f,0x30182df3,0xa65530ff,0xb0d02a41,0xaf28af28,
0xb0182df3,0xb018adf3,0x2a41b0d0,0x310f0000,0xaa41b0d0,0x3082ac98,0x2df33018,0xaf282f28,0x310f0000,0x310f,0xb10f0000,0x8000b10f,
0xaa4130d0,0xb082ac98,0x2df3b018,0x2f282f28,0xb018adf3,0x3018adf3,0x2a4130d0,0xb10f0000,0x3018adf3,0x265530ff,0xb0d0aa41,0x2f28af28,
0x2a4130d0,0xb0d0aa41,0x3018adf3,0x310f,0xb10f0000,0x2f28af28,0x310f,0xaf28af28,0x8000310f,0x8000310f,0x8000310f,0x8000310f,
0xadf3b018,0xac98b082,0xaa41b0d0,0xa655b0ff,0x30d02a41,0x30182df3,0x2df33018,0x2a4130d0,0xb0d02a41,0xb0ffa655,0xb018adf3,0xac98b082,
0x2df3b018,0x30d0aa41,0x30d02a41,0x2df33018,0x8000310f,0xaf282f28,0xb10f0000,0xaf28af28,0xadf3b018,0x2a41b0d0,0x30d0aa41,0x30182df3,
0x30d02a41,0x265530ff,0xb0182df3,0xb082ac98,0xb0d02a41,0xadf3b018,0x2df3b018,0x30d02a41,0x2df3b018,0x30822c98,0xaa4130d0,0xb0ffa655,
0x8000310f,0xb10f0000,0x8000b10f,0x310f8000,0xadf3b018,0x3082ac98,0x2a4130d0,0xb0ff2655,0x30d02a41,0xadf33018,0xadf3b018,0x30d0aa41,
0xb0d02a41,0x2655b0ff,0x30182df3,0xb0822c98,0x2df3b018,0x2a4130d0,0xb0d0aa41,0x3018adf3,0x8000310f,0xaf28af28,0x310f8000,0xaf282f28,
0x8000310f,0x8000310f,0x8000310f,0x8000310f,0x8000b10f,0x2655b0ff,0x2a41b0d0,0x2c98b082,0x8000310f,0xaa4130d0,0xadf33018,0xb0182df3,
0x8000b10f,0x2c98b082,0x3018adf3,0x30ffa655,0x8000310f,0xadf33018,0xb0d02a41,0xb0d0aa41,0x8000b10f,0x2f28af28,0x310f8000,0x2f282f28,
0x8000310f,0xb0182df3,0xb0d0aa41,0xaa41b0d0,0x8000b10f,0x3082ac98,0x30182df3,0xa65530ff,0x8000310f,0xb0d02a41,0xadf3b018,0x2df3b018,
0x8000b10f,0x30ffa655,0x2a4130d0,0xb0822c98,0x8000310f,0xb10f0000,0x8000b10f,0x310f8000,0x8000b10f,0x30ff2655,0xaa4130d0,0xb082ac98,
0x8000310f,0xb0d0aa41,0x2df3b018,0x2df33018,0x8000b10f,0x30822c98,0xb0182df3,0xa655b0ff,0x8000310f,0xb018adf3,0x30d0aa41,0xaa4130d0,
0x8000b10f,0x2f282f28,0xb10f0000,0x2f28af28,0x8000310f,0x8000310f,0x8000310f,0x8000310f,0x2df3b018,0x2f28af28,0x3018adf3,0x3082ac98,
0xb0d02a41,0xb10f0000,0xb0d0aa41,0xb018adf3,0x30d02a41,0x2f282f28,0x2a4130d0,0xa65530ff,0xadf3b018,0x8000b10f,0x2df3b018,0x30d0aa41,
0x8000310f,0xaf282f28,0xb10f0000,0xaf28af28,0x2df3b018,0x310f8000,0x2df33018,0xaa4130d0,0xb0d02a41,0xaf28af28,0x2a41b0d0,0x30ffa655,
0x30d02a41,0x8000310f,0xb0d02a41,0xadf3b018,0xadf3b018,0x2f28af28,0x30182df3,0xac983082,0x8000310f,0xb10f0000,0x8000b10f,0x310f8000,
0x2df3b018,0x2f282f28,0xb0182df3,0xac98b082,0xb0d02a41,0x8000b10f,0x30d02a41,0xadf33018,0x30d02a41,0xaf282f28,0xaa41b0d0,0x30ff2655,
0xadf3b018,0x310f8000,0xadf33018,0xaa41b0d0,0x8000310f,0xaf28af28,0x310f8000,0xaf282f28,0x8000310f,0x8000310f,0x8000310f,0x8000310f,
0x30d0aa41,0x30ffa655,0x310f8000,0x30ff2655,0xadf3b018,0xaa41b0d0,0x8000b10f,0x2a41b0d0,0xadf33018,0xb0822c98,0xb10f0000,0xb082ac98,
0x30d02a41,0x2df33018,0x8000310f,0xadf33018,0x8000b10f,0x2f28af28,0x310f8000,0x2f282f28,0xb0d02a41,0xb018adf3,0x8000b10f,0x3018adf3,
0x2df33018,0xac983082,0xb10f0000,0xac98b082,0x2df3b018,0x30d02a41,0x8000310f,0xb0d02a41,0xb0d0aa41,0x2655b0ff,0x310f8000,0x265530ff,
0x8000310f,0xb10f0000,0x8000b10f,0x310f8000,0x30d0aa41,0x265530ff,0xb10f0000,0x2655b0ff,0xadf3b018,0x30d0aa41,0x8000310f,0xb0d0aa41,
0xadf33018,0xac98b082,0x310f8000,0xac983082,0x30d02a41,0xb0182df3,0x8000b10f,0x30182df3,0x8000b10f,0x2f282f28,0xb10f0000,0x2f28af28,
0x310f0000,0x310f0000,0x310f0000,0x310f0000,0xb0182df3,0xb0822c98,0xb0d02a41,0xb0ff2655,0x2a41b0d0,0x2df3b018,0x3018adf3,0x30d0aa41,
0x2a4130d0,0xa65530ff,0xadf33018,0xb0822c98,0xb018adf3,0xaa41b0d0,0x2a41b0d0,0x3018adf3,0x310f0000,0x2f282f28,0x310f,0xaf282f28,
0xb0182df3,0xb0d0aa41,0xaa41b0d0,0x2df3b018,0x2a41b0d0,0x30ffa655,0x2df33018,0xac983082,0x2a4130d0,0xb0182df3,0xb018adf3,0x2a41b0d0,
0xb018adf3,0x2c98b082,0x30d02a41,0xa65530ff,0x310f0000,0x310f,0xb10f0000,0x8000b10f,0xb0182df3,0xac98b082,0x30d0aa41,0x265530ff,
0x2a41b0d0,0x30182df3,0xb0182df3,0xaa41b0d0,0x2a4130d0,0xb0ffa655,0x2df3b018,0x2c983082,0xb018adf3,0x30d0aa41,0xaa4130d0,0xadf3b018,
0x310f0000,0xaf282f28,0x8000b10f,0x2f282f28,0x310f0000,0x310f0000,0x310f0000,0x310f0000,0xb10f0000,0xb0ffa655,0xb0d0aa41,0xb082ac98,
0x310f0000,0x30d02a41,0x30182df3,0x2df33018,0xb10f0000,0xb082ac98,0xadf3b018,0xa655b0ff,0x310f0000,0x30182df3,0x2a4130d0,0xaa4130d0,
0xb10f0000,0xaf28af28,0x8000b10f,0x2f28af28,0x310f0000,0x2df33018,0xaa4130d0,0xb0d02a41,0xb10f0000,0xac98b082,0x2df3b018,0x30ff2655,
0x310f0000,0x2a4130d0,0xb0182df3,0xb018adf3,0xb10f0000,0xa655b0ff,0x30d0aa41,0x2c983082,0x310f0000,0x310f,0xb10f0000,0x8000b10f,
0xb10f0000,0x2655b0ff,0x30d02a41,0xac983082,0x310f0000,0xaa4130d0,0xb018adf3,0x3018adf3,0xb10f0000,0x2c98b082,0x2df33018,0xb0ff2655,
0x310f0000,0xadf33018,0xaa41b0d0,0x30d02a41,0xb10f0000,0x2f28af28,0x310f,0xaf28af28,0x310f0000,0x310f0000,0x310f0000,0x310f0000,
0xb018adf3,0xaf28af28,0xadf3b018,0xac98b082,0x2a4130d0,0x310f,0xaa4130d0,0xadf33018,0x2a41b0d0,0x2f28af28,0x30d0aa41,0x30ff2655,
0xb0182df3,0xb10f0000,0xb018adf3,0xaa41b0d0,0x310f0000,0x2f282f28,0x310f,0xaf282f28,0xb018adf3,0x8000b10f,0x3018adf3,0x30d02a41,
0x2a4130d0,0xaf282f28,0xb0d0aa41,0xa655b0ff,0x2a41b0d0,0x310f0000,0x2a4130d0,0xb0182df3,0xb0182df3,0xaf28af28,0x2df3b018,0x30822c98,
0x310f0000,0x310f,0xb10f0000,0x8000b10f,0xb018adf3,0x2f28af28,0x2df33018,0xb0822c98,0x2a4130d0,0xb10f0000,0x2a41b0d0,0x30182df3,
0x2a41b0d0,0x2f282f28,0xb0d02a41,0x2655b0ff,0xb0182df3,0x8000b10f,0x30182df3,0xb0d02a41,0x310f0000,0xaf282f28,0x8000b10f,0x2f282f28,
0x310f0000,0x310f0000,0x310f0000,0x310f0000,0xaa41b0d0,0xa655b0ff,0x8000b10f,0x2655b0ff,0xb0182df3,0xb0d02a41,0xb10f0000,0xb0d0aa41,
0x30182df3,0x2c983082,0x310f,0xac983082,0x2a41b0d0,0x3018adf3,0x310f0000,0x30182df3,0xb10f0000,0xaf28af28,0x8000b10f,0x2f28af28,
0x2a4130d0,0xadf33018,0xb10f0000,0xadf3b018,0x3018adf3,0x30822c98,0x310f,0xb0822c98,0xb018adf3,0x2a41b0d0,0x310f0000,0x2a4130d0,
0xaa4130d0,0xb0ffa655,0x8000b10f,0x30ffa655,0x310f0000,0x310f,0xb10f0000,0x8000b10f,0xaa41b0d0,0x30ffa655,0x310f,0xb0ffa655,
0xb0182df3,0xaa41b0d0,0x310f0000,0xaa4130d0,0x30182df3,0xb0822c98,0x8000b10f,0x30822c98,0x2a41b0d0,0x2df33018,0xb10f0000,0x2df3b018,
0xb10f0000,0x2f28af28,0x310f,0xaf28af28,0x8000310f,0x8000310f,0x8000310f,0x8000310f,0x30d02a41,0x30822c98,0x30182df3,0x2f282f28,
0x2df3b018,0x3018adf3,0x30d0aa41,0x310f8000,0xadf3b018,0xa655b0ff,0x2a41b0d0,0x2f28af28,0xb0d02a41,0xb0d0aa41,0xadf3b018,0x8000b10f,
0x8000310f,0xaf282f28,0xb10f0000,0xaf28af28,0x30d02a41,0x2a4130d0,0xadf33018,0xb10f0000,0x2df3b018,0x30ff2655,0x2a4130d0,0xaf282f28,
0xadf3b018,0x2df3b018,0x30d02a41,0x8000310f,0xb0d02a41,0xac98b082,0x3018adf3,0x2f282f28,0x8000310f,0xb10f0000,0x8000b10f,0x310f8000,
0x30d02a41,0xac983082,0xb018adf3,0x2f28af28,0x2df3b018,0x2df33018,0xb0d02a41,0x8000b10f,0xadf3b018,0x30ffa655,0xaa4130d0,0xaf28af28,
0xb0d02a41,0x2a41b0d0,0x2df33018,0xb10f0000,0x8000310f,0xaf28af28,0x310f8000,0xaf282f28,0x8000310f,0x8000310f,0x8000310f,0x8000310f,
0x2df33018,0x2c983082,0x2a4130d0,0x265530ff,0x30d02a41,0x30182df3,0x2df33018,0x2a4130d0,0x30d0aa41,0x30ff2655,0x30182df3,0x2c983082,
0x2df3b018,0x30d0aa41,0x30d02a41,0x2df33018,0x8000b10f,0x2f28af28,0x310f8000,0x2f282f28,0xadf3b018,0x2a41b0d0,0x30d0aa41,0x30182df3,
0xb0d0aa41,0xa655b0ff,0x3018adf3,0x30822c98,0xb0d02a41,0xadf3b018,0x2df3b018,0x30d02a41,0xadf33018,0xb082ac98,0x2a41b0d0,0x30ff2655,
0x8000310f,0xb10f0000,0x8000b10f,0x310f8000,0x2df33018,0xb0822c98,0xaa41b0d0,0x30ffa655,0x30d02a41,0xadf33018,0xadf3b018,0x30d0aa41,
0x30d0aa41,0xa65530ff,0xb018adf3,0x3082ac98,0x2df3b018,0x2a4130d0,0xb0d0aa41,0x3018adf3,0x8000b10f,0x2f282f28,0xb10f0000,0x2f28af28,
0x0,0x0,0x0,0x0,0x0,0x0,0x0,0x0,0x0,0x0,0x0,0x0,
0x0,0x0,0x0,0x0,0x0,0x0,0x0,0x0,0x0,0x0,0x0,0x0,
0x0,0x0,0x0,0x0,0x0,0x0,0x0,0x0,0x0,0x0,0x0,0x0,
0x0,0x0,0x0,0x0,0x0,0x0,0x0,0x0,0x0,0x0,0x0,0x0,
0x0,0x0,0x0,0x0,0x0,0x0,0x0,0x0,0x0,0x0,0x0,0x0,
0x0,0x0,0x0,0x0,0x0,0x0,0x0,0x0,0x0,0x0,0x0,0x0,
0x0,0x0,0x0,0x0,0x0,0x0,0x0,0x0,0x0,0x0,0x0,0x0,
0x0,0x0,0x0,0x0,0x0,0x0,0x0,0x0,0x0,0x0,0x0,0x0,
0x0,0x0,0x0,0x0,0x0,0x0,0x0,0x0,0x0,0x0,0x0,0x0,
0x0,0x0,0x0,0x0,0x0,0x0,0x0,0x0,0x0,0x0,0x0,0x0,
0x0,0x0,0x0,0x0,0x0,0x0,0x0,0x0,0x310f0000,0x310f0000,0x310f0000,0x310f0000,
0x2a41b0d0,0x2c98b082,0x2df3b018,0x2f28af28,0xb018adf3,0xadf3b018,0xaa41b0d0,0x8000b10f,0xb0182df3,0xb0ff2655,0xb0d0aa41,0xaf28af28,
0x2a4130d0,0xaa4130d0,0xb0182df3,0xb10f0000,0x310f0000,0x2f282f28,0x310f,0xaf282f28,0x2a41b0d0,0x30d0aa41,0x30182df3,0x310f,
0xb018adf3,0x2655b0ff,0x30d0aa41,0x2f282f28,0xb0182df3,0xb018adf3,0x2a41b0d0,0x310f0000,0x2a4130d0,0xb0822c98,0xadf3b018,0x2f28af28,
0x310f0000,0x310f,0xb10f0000,0x8000b10f,0x2a41b0d0,0x30822c98,0xadf33018,0xaf28af28,0xb018adf3,0x3018adf3,0x2a4130d0,0xb10f0000,
0xb0182df3,0xa655b0ff,0x30d02a41,0xaf282f28,0x2a4130d0,0xb0d0aa41,0x3018adf3,0x310f,0x310f0000,0xaf282f28,0x8000b10f,0x2f282f28,
0x310f0000,0x310f0000,0x310f0000,0x310f0000,0x3018adf3,0x3082ac98,0x30d0aa41,0x30ffa655,0x2a41b0d0,0x2df3b018,0x3018adf3,0x30d0aa41,
0xaa41b0d0,0x2655b0ff,0x2df3b018,0x3082ac98,0xb018adf3,0xaa41b0d0,0x2a41b0d0,0x3018adf3,0xb10f0000,0xaf28af28,0x8000b10f,0x2f28af28,
0xb0182df3,0xb0d0aa41,0xaa41b0d0,0x2df3b018,0xaa4130d0,0xb0ff2655,0xadf3b018,0x2c98b082,0x2a4130d0,0xb0182df3,0xb018adf3,0x2a41b0d0,
0x30182df3,0xac983082,0xb0d0aa41,0x2655b0ff,0x310f0000,0x310f,0xb10f0000,0x8000b10f,0x3018adf3,0x2c983082,0xb0d02a41,0xa655b0ff,
0x2a41b0d0,0x30182df3,0xb0182df3,0xaa41b0d0,0xaa41b0d0,0x30ff2655,0xadf33018,0xac98b082,0xb018adf3,0x30d0aa41,0xaa4130d0,0xadf3b018,
0xb10f0000,0x2f28af28,0x310f,0xaf28af28,0x0,0x0,0x0,0x0,0x0,0x0,0x0,0x0,
0x0,0x0,0x0,0x0,0x0,0x0,0x0,0x0,0x0,0x0,0x0,0x0,
0x0,0x0,0x0,0x0,0x0,0x0,0x0,0x0,0x0,0x0,0x0,0x0,
0x0,0x0,0x0,0x0,0x0,0x0,0x0,0x0,0x0,0x0,0x0,0x0,
0x0,0x0,0x0,0x0,0x0,0x0,0x0,0x0,0x0,0x0,0x0,0x0,
0x0,0x0,0x0,0x0,0x0,0x0,0x0,0x0,0x0,0x0,0x0,0x0,
0x0,0x0,0x0,0x0,0x0,0x0,0x0,0x0,0x0,0x0,0x0,0x0,
0x0,0x0,0x0,0x0,0x0,0x0,0x0,0x0,0x0,0x0,0x0,0x0,
0x0,0x0,0x0,0x0,0x0,0x0,0x0,0x0,0x0,0x0,0x0,0x0,
0x0,0x0,0x0,0x0,0x0,0x0,0x0,0x0,0x0,0x0,0x0,0x0,
0x0,0x0,0x0,0x0,0x0,0x0,0x0,0x0,0x0,0x0,0x0,0x0,
0x8000310f,0xadf3b018,0x30d02a41,0xb0d02a41,0x8000310f,0xac98b082,0x30182df3,0xb0ffa655,0x8000310f,0xaa41b0d0,0x2df33018,0xb018adf3,
0x8000310f,0xa655b0ff,0x2a4130d0,0xac98b082,0x8000310f,0x8000b10f,0x8000310f,0x8000b10f,0x8000310f,0x2655b0ff,0xaa4130d0,0x2c98b082,
0x8000310f,0x2a41b0d0,0xadf33018,0x3018adf3,0x8000310f,0x2c98b082,0xb0182df3,0x30ffa655,0x8000310f,0x2df3b018,0xb0d02a41,0x30d02a41,
0x8000310f,0x2f28af28,0xb10f0000,0x2f282f28,0x8000310f,0x3018adf3,0xb0d0aa41,0x2a4130d0,0x8000310f,0x3082ac98,0xb018adf3,0xa65530ff,
0x8000310f,0x30d0aa41,0xadf3b018,0xadf33018,0x8000310f,0x30ffa655,0xaa41b0d0,0xb0822c98,0x8000310f,0x310f8000,0x8000b10f,0xb10f0000,
0x8000310f,0x30ff2655,0x2a41b0d0,0xb082ac98,0x2df3b018,0x8000310f,0xadf3b018,0x30d02a41,0x30d0aa41,0xaf282f28,0x2a41b0d0,0x265530ff,
0x30d02a41,0xb10f0000,0x30d0aa41,0xb0182df3,0x2df33018,0xaf28af28,0x30182df3,0xb082ac98,0x8000310f,0x8000b10f,0x8000310f,0x8000b10f,
0xadf33018,0x2f28af28,0xb0182df3,0x3082ac98,0xb0d02a41,0x310f8000,0xb0d0aa41,0x30182df3,0xb0d0aa41,0x2f282f28,0xaa41b0d0,0xa65530ff,
0xadf3b018,0x8000310f,0x2df3b018,0xb0d02a41,0x8000b10f,0xaf282f28,0x310f8000,0xaf28af28,0x2df3b018,0xb10f0000,0x2df33018,0x2a41b0d0,
0x30d0aa41,0xaf28af28,0xaa4130d0,0x30ffa655,0x30d02a41,0x8000b10f,0xb0d02a41,0x2df33018,0x2df33018,0x2f28af28,0xb018adf3,0xac983082,
0x8000310f,0x310f8000,0x8000b10f,0xb10f0000,0xadf33018,0x2f282f28,0x3018adf3,0xac98b082,0xb0d02a41,0x2df3b018,0x8000310f,0xadf3b018,
0xadf3b018,0x30822c98,0xb10f0000,0x3082ac98,0x2df3b018,0xaa4130d0,0x8000b10f,0x2a4130d0,0x30d02a41,0xb0ffa655,0x310f8000,0xb0ff2655,
0x8000310f,0x8000b10f,0x8000310f,0x8000b10f,0xb0d02a41,0x30ffa655,0xb10f0000,0x30ff2655,0xadf3b018,0x2a4130d0,0x8000b10f,0xaa4130d0,
0x2df3b018,0xb0822c98,0x310f8000,0xb082ac98,0x30d02a41,0xadf3b018,0x8000310f,0x2df3b018,0x8000310f,0x2f28af28,0xb10f0000,0x2f282f28,
0xb0d02a41,0x30182df3,0x8000b10f,0xb0182df3,0xadf3b018,0xac983082,0x310f8000,0xac98b082,0x2df3b018,0xb0d0aa41,0x8000310f,0x30d0aa41,
0x30d02a41,0x2655b0ff,0xb10f0000,0x265530ff,0x8000310f,0x310f8000,0x8000b10f,0xb10f0000,0xb0d02a41,0x265530ff,0x310f8000,0x2655b0ff,
0x30d02a41,0xb0d02a41,0x2df3b018,0x8000310f,0xadf33018,0x2655b0ff,0x2a4130d0,0xaf28af28,0xadf3b018,0x30182df3,0xb0d0aa41,0x310f8000,
0x30d0aa41,0xb0822c98,0x3018adf3,0xaf282f28,0x8000310f,0x8000b10f,0x8000310f,0x8000b10f,0xb0d0aa41,0x30822c98,0xb018adf3,0x2f282f28,
0x2df3b018,0xb0182df3,0x30d0aa41,0xb10f0000,0x2df33018,0xa655b0ff,0xaa4130d0,0x2f28af28,0xb0d02a41,0x30d02a41,0xadf3b018,0x8000310f,
0x8000b10f,0xaf282f28,0x310f8000,0xaf28af28,0x30d02a41,0xaa41b0d0,0xadf33018,0x310f8000,0xadf33018,0x30ff2655,0xaa41b0d0,0xaf282f28,
0xadf3b018,0xadf33018,0x30d02a41,0x8000b10f,0x30d0aa41,0xac98b082,0xb0182df3,0x2f282f28,0x8000310f,0x310f8000,0x8000b10f,0xb10f0000,
0xb0d0aa41,0xac983082,0x30182df3,0x2f28af28,0x310f0000,0xb0182df3,0x2a41b0d0,0x2a4130d0,0x310f0000,0xb0822c98,0x2df3b018,0xa65530ff,
0x310f0000,0xb0d02a41,0x3018adf3,0xadf33018,0x310f0000,0xb0ff2655,0x30d0aa41,0xb0822c98,0x310f0000,0xb10f0000,0x310f0000,0xb10f0000,
0x310f0000,0xb0ffa655,0x30d02a41,0xb082ac98,0x310f0000,0xb0d0aa41,0x30182df3,0xadf3b018,0x310f0000,0xb082ac98,0x2df33018,0xa655b0ff,
0x310f0000,0xb018adf3,0x2a4130d0,0x2a41b0d0,0x310f0000,0xaf28af28,0x310f,0x2f28af28,0x310f0000,0xadf3b018,0xaa4130d0,0x30d0aa41,
0x310f0000,0xac98b082,0xadf33018,0x30ff2655,0x310f0000,0xaa41b0d0,0xb0182df3,0x30182df3,0x310f0000,0xa655b0ff,0xb0d02a41,0x2c983082,
0x310f0000,0x8000b10f,0xb10f0000,0x310f,0x310f0000,0x2655b0ff,0xb0d0aa41,0xac983082,0xb018adf3,0x310f0000,0xb0182df3,0x2a41b0d0,
0xaa41b0d0,0x2f282f28,0xb0d0aa41,0x30ffa655,0x2a41b0d0,0x310f,0xaa41b0d0,0x2df33018,0x3018adf3,0xaf282f28,0x2df3b018,0xac983082,
0x310f0000,0xb10f0000,0x310f0000,0xb10f0000,0x30182df3,0xaf28af28,0x2df33018,0xac98b082,0x2a4130d0,0x8000b10f,0xaa4130d0,0x2df3b018,
0xaa4130d0,0x2f28af28,0xb0d02a41,0x30ff2655,0xb0182df3,0x310f0000,0xb018adf3,0x2a4130d0,0xb10f0000,0x2f282f28,0x8000b10f,0xaf282f28,
0xb018adf3,0x310f,0x3018adf3,0xb0d0aa41,0xaa41b0d0,0xaf282f28,0x30d02a41,0xa655b0ff,0x2a41b0d0,0xb10f0000,0x2a4130d0,0x3018adf3,
0x3018adf3,0xaf28af28,0xadf33018,0x30822c98,0x310f0000,0x8000b10f,0xb10f0000,0x310f,0x30182df3,0x2f28af28,0xadf3b018,0xb0822c98,
0x2a4130d0,0xb018adf3,0x310f0000,0xb0182df3,0xb0182df3,0x2c98b082,0x310f,0xac98b082,0xb018adf3,0x30d02a41,0xb10f0000,0x30d0aa41,
0x2a41b0d0,0xa65530ff,0x8000b10f,0x265530ff,0x310f0000,0xb10f0000,0x310f0000,0xb10f0000,0x2a4130d0,0xa655b0ff,0x310f,0x2655b0ff,
0xb0182df3,0x30d0aa41,0xb10f0000,0x30d02a41,0xb018adf3,0x2c983082,0x8000b10f,0xac983082,0x2a41b0d0,0xb0182df3,0x310f0000,0xb018adf3,
0x310f0000,0xaf28af28,0x310f,0x2f28af28,0x2a4130d0,0x2df3b018,0xb10f0000,0x2df33018,0xb0182df3,0x30822c98,0x8000b10f,0xb0822c98,
0xb018adf3,0xaa4130d0,0x310f0000,0xaa41b0d0,0x2a41b0d0,0xb0ffa655,0x310f,0x30ffa655,0x310f0000,0x8000b10f,0xb10f0000,0x310f,
0x2a4130d0,0x30ffa655,0x8000b10f,0xb0ffa655,0x2a41b0d0,0x2a4130d0,0xb018adf3,0x310f0000,0x30182df3,0xb0ffa655,0x30d0aa41,0xaf282f28,
0xb0182df3,0x2df3b018,0xaa4130d0,0x8000b10f,0xaa41b0d0,0x2c983082,0xadf3b018,0x2f282f28,0x310f0000,0xb10f0000,0x310f0000,0xb10f0000,
0xaa4130d0,0x2c98b082,0xadf33018,0x2f28af28,0xb018adf3,0x2df33018,0xaa41b0d0,0x310f,0x3018adf3,0xb0ff2655,0x30d02a41,0xaf28af28,
0x2a4130d0,0x2a41b0d0,0xb0182df3,0x310f0000,0xb10f0000,0x2f282f28,0x8000b10f,0xaf282f28,0x2a41b0d0,0xb0d02a41,0x30182df3,0x8000b10f,
0x30182df3,0x2655b0ff,0xb0d02a41,0x2f282f28,0xb0182df3,0x30182df3,0x2a41b0d0,0xb10f0000,0xaa41b0d0,0xb0822c98,0x2df33018,0x2f28af28,
0x310f0000,0x8000b10f,0xb10f0000,0x310f,0xaa4130d0,0x30822c98,0x2df3b018,0xaf28af28,0xadf3b018,0x30d02a41,0xb0d02a41,0x2df3b018,
0x30d02a41,0xb0ff2655,0x3018adf3,0xac983082,0xb0d02a41,0x3018adf3,0xadf33018,0x2a41b0d0,0x2df3b018,0xac983082,0x2a41b0d0,0xa65530ff,
0x8000310f,0x8000b10f,0x8000310f,0x8000b10f,0xadf3b018,0x2c983082,0xaa41b0d0,0x265530ff,0x30d02a41,0xb018adf3,0x2df33018,0xaa41b0d0,
0xb0d02a41,0x30ff2655,0xb018adf3,0x2c983082,0x2df3b018,0xb0d02a41,0x30d02a41,0xadf3b018,0x8000310f,0x2f28af28,0xb10f0000,0x2f282f28,
0xadf3b018,0xaa4130d0,0x30d0aa41,0xb018adf3,0x30d02a41,0xa655b0ff,0xb0182df3,0x30822c98,0xb0d02a41,0x2df33018,0x2df3b018,0xb0d0aa41,
0x2df3b018,0xb082ac98,0xaa4130d0,0x30ff2655,0x8000310f,0x310f8000,0x8000b10f,0xb10f0000,0xadf3b018,0xb0822c98,0x2a4130d0,0x30ffa655,
0x8000310f,0xadf3b018,0x30d02a41,0xb0d02a41,0x8000b10f,0x2c983082,0xb018adf3,0x30ff2655,0x8000310f,0xaa41b0d0,0x2df33018,0xb018adf3,
0x8000b10f,0x265530ff,0xaa41b0d0,0x2c983082,0x8000310f,0x8000b10f,0x8000310f,0x8000b10f,0x8000b10f,0xa65530ff,0x2a41b0d0,0xac983082,
0x8000310f,0x2a41b0d0,0xadf33018,0x3018adf3,0x8000b10f,0xac983082,0x3018adf3,0xb0ff2655,0x8000310f,0x2df3b018,0xb0d02a41,0x30d02a41,
0x8000b10f,0xaf282f28,0x310f8000,0xaf28af28,0x8000310f,0x3018adf3,0xb0d0aa41,0x2a4130d0,0x8000b10f,0xb0822c98,0x30182df3,0x2655b0ff,
0x8000310f,0x30d0aa41,0xadf3b018,0xadf33018,0x8000b10f,0xb0ff2655,0x2a4130d0,0x3082ac98,0x8000310f,0x310f8000,0x8000b10f,0xb10f0000,
0x8000b10f,0xb0ffa655,0xaa4130d0,0x30822c98,0x2df3b018,0x8000310f,0xadf3b018,0x30d02a41,0xb0d02a41,0x2f28af28,0xaa4130d0,0xa655b0ff,
0x30d02a41,0xb10f0000,0x30d0aa41,0xb0182df3,0xadf3b018,0x2f282f28,0xb018adf3,0x30822c98,0x8000310f,0x8000b10f,0x8000310f,0x8000b10f,
0x2df3b018,0xaf282f28,0x3018adf3,0xb0822c98,0xb0d02a41,0x310f8000,0xb0d0aa41,0x30182df3,0x30d02a41,0xaf28af28,0x2a4130d0,0x2655b0ff,
0xadf3b018,0x8000310f,0x2df3b018,0xb0d02a41,0x8000310f,0x2f28af28,0xb10f0000,0x2f282f28,0x2df3b018,0xb10f0000,0x2df33018,0x2a41b0d0,
0xb0d02a41,0x2f282f28,0x2a41b0d0,0xb0ff2655,0x30d02a41,0x8000b10f,0xb0d02a41,0x2df33018,0xadf3b018,0xaf282f28,0x30182df3,0x2c98b082,
0x8000310f,0x310f8000,0x8000b10f,0xb10f0000,0x2df3b018,0xaf28af28,0xb0182df3,0x2c983082,0xb0d02a41,0x2df3b018,0x8000310f,0xadf3b018,
0x2df33018,0xb082ac98,0x310f8000,0xb0822c98,0x2df3b018,0xaa4130d0,0x8000b10f,0x2a4130d0,0xb0d0aa41,0x30ff2655,0xb10f0000,0x30ffa655,
0x8000310f,0x8000b10f,0x8000310f,0x8000b10f,0x30d0aa41,0xb0ff2655,0x310f8000,0xb0ffa655,0xadf3b018,0x2a4130d0,0x8000b10f,0xaa4130d0,
0xadf33018,0x3082ac98,0xb10f0000,0x30822c98,0x30d02a41,0xadf3b018,0x8000310f,0x2df3b018,0x8000b10f,0xaf282f28,0x310f8000,0xaf28af28,
0xb0d02a41,0x30182df3,0x8000b10f,0xb0182df3,0x2df33018,0x2c98b082,0xb10f0000,0x2c983082,0x2df3b018,0xb0d0aa41,0x8000310f,0x30d0aa41,
0xb0d0aa41,0xa65530ff,0x310f8000,0xa655b0ff,0x8000310f,0x310f8000,0x8000b10f,0xb10f0000,0x30d0aa41,0xa655b0ff,0xb10f0000,0xa65530ff,
0xb0182df3,0x2a41b0d0,0x2a4130d0,0xb018adf3,0x2a41b0d0,0x265530ff,0xadf3b018,0x30822c98,0x2a4130d0,0xadf3b018,0x30182df3,0xb0d0aa41,
0xb018adf3,0x30822c98,0xb0d0aa41,0x30ff2655,0x310f0000,0xb10f0000,0x310f0000,0xb10f0000,0xb0182df3,0x3082ac98,0xb0d02a41,0x30ffa655,
0x2a41b0d0,0xadf33018,0x3018adf3,0xb0d02a41,0x2a4130d0,0x2655b0ff,0xadf33018,0x3082ac98,0xb018adf3,0x2a4130d0,0x2a41b0d0,0xb0182df3,
0x310f0000,0xaf28af28,0x310f,0x2f28af28,0xb0182df3,0x30d02a41,0xaa41b0d0,0xadf33018,0x2a41b0d0,0xb0ff2655,0x2df33018,0x2c98b082,
0x2a4130d0,0x3018adf3,0xb018adf3,0xaa4130d0,0xb018adf3,0xac983082,0x30d02a41,0x2655b0ff,0x310f0000,0x8000b10f,0xb10f0000,0x310f,
0xb0182df3,0x2c983082,0x30d0aa41,0xa655b0ff,0x310f0000,0xb0182df3,0x2a41b0d0,0x2a4130d0,0xb10f0000,0x3082ac98,0xadf33018,0x2655b0ff,
0x310f0000,0xb0d02a41,0x3018adf3,0xadf33018,0xb10f0000,0x30ffa655,0xb0d02a41,0x3082ac98,0x310f0000,0xb10f0000,0x310f0000,0xb10f0000,
0xb10f0000,0x30ff2655,0xb0d0aa41,0x30822c98,0x310f0000,0xb0d0aa41,0x30182df3,0xadf3b018,0xb10f0000,0x30822c98,0xadf3b018,0x265530ff,
0x310f0000,0xb018adf3,0x2a4130d0,0x2a41b0d0,0xb10f0000,0x2f282f28,0x8000b10f,0xaf282f28,0x310f0000,0xadf3b018,0xaa4130d0,0x30d0aa41,
0xb10f0000,0x2c983082,0x2df3b018,0xb0ffa655,0x310f0000,0xaa41b0d0,0xb0182df3,0x30182df3,0xb10f0000,0x265530ff,0x30d0aa41,0xac98b082,
0x310f0000,0x8000b10f,0xb10f0000,0x310f,0xb10f0000,0xa65530ff,0x30d02a41,0x2c98b082,0xb018adf3,0x310f0000,0xb0182df3,0x2a41b0d0,
0x2a4130d0,0xaf28af28,0x30d02a41,0xb0ff2655,0x2a41b0d0,0x310f,0xaa41b0d0,0x2df33018,0xb0182df3,0x2f28af28,0xadf33018,0x2c98b082,
0x310f0000,0xb10f0000,0x310f0000,0xb10f0000,0xb018adf3,0x2f282f28,0xadf3b018,0x2c983082,0x2a4130d0,0x8000b10f,0xaa4130d0,0x2df3b018,
0x2a41b0d0,0xaf282f28,0x30d0aa41,0xb0ffa655,0xb0182df3,0x310f0000,0xb018adf3,0x2a4130d0,0x310f0000,0xaf28af28,0x310f,0x2f28af28,
0xb018adf3,0x310f,0x3018adf3,0xb0d0aa41,0x2a4130d0,0x2f28af28,0xb0d0aa41,0x265530ff,0x2a41b0d0,0xb10f0000,0x2a4130d0,0x3018adf3,
0xb0182df3,0x2f282f28,0x2df3b018,0xb082ac98,0x310f0000,0x8000b10f,0xb10f0000,0x310f,0xb018adf3,0xaf282f28,0x2df33018,0x3082ac98,
0x2a4130d0,0xb018adf3,0x310f0000,0xb0182df3,0x3018adf3,0xac983082,0x8000b10f,0x2c983082,0xb018adf3,0x30d02a41,0xb10f0000,0x30d0aa41,
0xaa4130d0,0x2655b0ff,0x310f,0xa655b0ff,0x310f0000,0xb10f0000,0x310f0000,0xb10f0000,0xaa41b0d0,0x265530ff,0x8000b10f,0xa65530ff,
0xb0182df3,0x30d0aa41,0xb10f0000,0x30d02a41,0x30182df3,0xac98b082,0x310f,0x2c98b082,0x2a41b0d0,0xb0182df3,0x310f0000,0xb018adf3,
0xb10f0000,0x2f282f28,0x8000b10f,0xaf282f28,0x2a4130d0,0x2df3b018,0xb10f0000,0x2df33018,0x3018adf3,0xb082ac98,0x310f,0x3082ac98,
0xb018adf3,0xaa4130d0,0x310f0000,0xaa41b0d0,0xaa4130d0,0x30ff2655,0x8000b10f,0xb0ff2655,0x310f0000,0x8000b10f,0xb10f0000,0x310f,
0xaa41b0d0,0xb0ff2655,0x310f,0x30ff2655,0x30d02a41,0xb0d02a41,0x2df3b018,0x8000310f,0x2df3b018,0xa65530ff,0xaa41b0d0,0x2f282f28,
0xadf3b018,0x30182df3,0xb0d0aa41,0x310f8000,0xb0d02a41,0x3082ac98,0xb0182df3,0x2f28af28,0x8000310f,0x8000b10f,0x8000310f,0x8000b10f,
0x30d02a41,0xb082ac98,0x30182df3,0xaf28af28,0x2df3b018,0xb0182df3,0x30d0aa41,0xb10f0000,0xadf3b018,0x265530ff,0x2a41b0d0,0xaf282f28,
0xb0d02a41,0x30d02a41,0xadf3b018,0x8000310f,0x8000310f,0x2f28af28,0xb10f0000,0x2f282f28,0x30d02a41,0xaa41b0d0,0xadf33018,0x310f8000,
0x2df3b018,0xb0ffa655,0x2a4130d0,0x2f28af28,0xadf3b018,0xadf33018,0x30d02a41,0x8000b10f,0xb0d02a41,0x2c983082,0x3018adf3,0xaf28af28,
0x8000310f,0x310f8000,0x8000b10f,0xb10f0000,0x30d02a41,0x2c98b082,0xb018adf3,0xaf282f28,0xadf3b018,0x30d02a41,0xb0d02a41,0x2df3b018,
0xb0d0aa41,0x30ffa655,0xb0182df3,0x2c98b082,0xb0d02a41,0x3018adf3,0xadf33018,0x2a41b0d0,0xadf33018,0x2c98b082,0xaa4130d0,0x2655b0ff,
0x8000310f,0x8000b10f,0x8000310f,0x8000b10f,0x2df33018,0xac98b082,0x2a4130d0,0xa655b0ff,0x30d02a41,0xb018adf3,0x2df33018,0xaa41b0d0,
0x30d0aa41,0xb0ffa655,0x30182df3,0xac98b082,0x2df3b018,0xb0d02a41,0x30d02a41,0xadf3b018,0x8000b10f,0xaf282f28,0x310f8000,0xaf28af28,
0xadf3b018,0xaa4130d0,0x30d0aa41,0xb018adf3,0xb0d0aa41,0x265530ff,0x3018adf3,0xb082ac98,0xb0d02a41,0x2df33018,0x2df3b018,0xb0d0aa41,
0xadf33018,0x30822c98,0x2a41b0d0,0xb0ffa655,0x8000310f,0x310f8000,0x8000b10f,0xb10f0000,0x2df33018,0x3082ac98,0xaa41b0d0,0xb0ff2655,
0x0,0x0,0x0,0x0,0x0,0x0,0x0,0x0,0x0,0x0,0x0,0x0,
0x0,0x0,0x0,0x0,0x0,0x0,0x0,0x0,0x0,0x0,0x0,0x0,
0x0,0x0,0x0,0x0,0x0,0x0,0x0,0x0,0x0,0x0,0x0,0x0,
0x0,0x0,0x0,0x0,0x0,0x0,0x0,0x0,0x0,0x0,0x0,0x0,
0x0,0x0,0x0,0x0,0x0,0x0,0x0,0x0,0x0,0x0,0x0,0x0,
0x0,0x0,0x0,0x0,0x0,0x0,0x0,0x0,0x0,0x0,0x0,0x0,
0x0,0x0,0x0,0x0,0x0,0x0,0x0,0x0,0x0,0x0,0x0,0x0,
0x0,0x0,0x0,0x0,0x0,0x0,0x0,0x0,0x0,0x0,0x0,0x0,
0x0,0x0,0x0,0x0,0x0,0x0,0x0,0x0,0x0,0x0,0x0,0x0,
0x0,0x0,0x0,0x0,0x0,0x0,0x0,0x0,0x0,0x0,0x0,0x0,
0x0,0x0,0x0,0x0,0x0,0x0,0x0,0x0,0x2a41b0d0,0x2a4130d0,0xb018adf3,0x310f0000,
0xb018adf3,0x30ff2655,0xb0d02a41,0x2f28af28,0xb0182df3,0x2df3b018,0xaa4130d0,0x8000b10f,0x2a4130d0,0xac98b082,0x2df33018,0xaf28af28,
0x310f0000,0xb10f0000,0x310f0000,0xb10f0000,0x2a41b0d0,0xac983082,0x2df3b018,0xaf282f28,0xb018adf3,0x2df33018,0xaa41b0d0,0x310f,
0xb0182df3,0x30ffa655,0xb0d0aa41,0x2f282f28,0x2a4130d0,0x2a41b0d0,0xb0182df3,0x310f0000,0x310f0000,0xaf28af28,0x310f,0x2f28af28,
0x2a41b0d0,0xb0d02a41,0x30182df3,0x8000b10f,0xb018adf3,0xa65530ff,0x30d0aa41,0xaf28af28,0xb0182df3,0x30182df3,0x2a41b0d0,0xb10f0000,
0x2a4130d0,0x3082ac98,0xadf3b018,0xaf282f28,0x310f0000,0x8000b10f,0xb10f0000,0x310f,0x2a41b0d0,0xb082ac98,0xadf33018,0x2f282f28,
0xb0182df3,0x2a41b0d0,0x2a4130d0,0xb018adf3,0xaa4130d0,0xa655b0ff,0x2df33018,0xb082ac98,0x2a4130d0,0xadf3b018,0x30182df3,0xb0d0aa41,
0x30182df3,0xb082ac98,0x30d02a41,0xb0ffa655,0x310f0000,0xb10f0000,0x310f0000,0xb10f0000,0x3018adf3,0xb0822c98,0x30d0aa41,0xb0ff2655,
0x2a41b0d0,0xadf33018,0x3018adf3,0xb0d02a41,0xaa41b0d0,0xa65530ff,0x2df3b018,0xb0822c98,0xb018adf3,0x2a4130d0,0x2a41b0d0,0xb0182df3,
0xb10f0000,0x2f282f28,0x8000b10f,0xaf282f28,0xb0182df3,0x30d02a41,0xaa41b0d0,0xadf33018,0xaa4130d0,0x30ffa655,0xadf3b018,0xac983082,
0x2a4130d0,0x3018adf3,0xb018adf3,0xaa4130d0,0x30182df3,0x2c98b082,0xb0d0aa41,0xa65530ff,0x310f0000,0x8000b10f,0xb10f0000,0x310f,
0x3018adf3,0xac98b082,0xb0d02a41,0x265530ff,0x0,0x0,0x0,0x0,0x0,0x0,0x0,0x0,
0x0,0x0,0x0,0x0,0x0,0x0,0x0,0x0,0x0,0x0,0x0,0x0,
0x0,0x0,0x0,0x0,0x0,0x0,0x0,0x0,0x0,0x0,0x0,0x0,
0x0,0x0,0x0,0x0,0x0,0x0,0x0,0x0,0x0,0x0,0x0,0x0,
0x0,0x0,0x0,0x0,0x0,0x0,0x0,0x0,0x0,0x0,0x0,0x0,
0x0,0x0,0x0,0x0,0x0,0x0,0x0,0x0,0x0,0x0,0x0,0x0,
0x0,0x0,0x0,0x0,0x0,0x0,0x0,0x0,0x0,0x0,0x0,0x0,
0x0,0x0,0x0,0x0,0x0,0x0,0x0,0x0,0x0,0x0,0x0,0x0,
0x0,0x0,0x0,0x0,0x0,0x0,0x0,0x0,0x0,0x0,0x0,0x0,
0x0,0x0,0x0,0x0,0x0,0x0,0x0,0x0,0x0,0x0,0x0,0x0,
0x0,0x0,0x0,0x0,0x0,0x0,0x0,0x0,0x0,0x0,0x0,0x0,
0x8000310f,0x2df3b018,0xb0d02a41,0x30d02a41,0x8000310f,0x2f28af28,0xb10f0000,0x2f282f28,0x8000310f,0x3018adf3,0xb0d0aa41,0x2a4130d0,
0x8000310f,0x3082ac98,0xb018adf3,0xa65530ff,0x8000310f,0x30d0aa41,0xadf3b018,0xadf33018,0x8000310f,0x30ffa655,0xaa41b0d0,0xb0822c98,
0x8000310f,0x310f8000,0x8000b10f,0xb10f0000,0x8000310f,0x30ff2655,0x2a41b0d0,0xb082ac98,0x8000310f,0x30d02a41,0x2df3b018,0xadf3b018,
0x8000310f,0x30822c98,0x3018adf3,0xa655b0ff,0x8000310f,0x30182df3,0x30d0aa41,0x2a41b0d0,0x8000310f,0x2f282f28,0x310f8000,0x2f28af28,
0x8000310f,0x2df33018,0x30d02a41,0x30d0aa41,0x8000310f,0x2c983082,0x30182df3,0x30ff2655,0x8000310f,0x2a4130d0,0x2df33018,0x30182df3,
0x8000310f,0x265530ff,0x2a4130d0,0x2c983082,0xadf3b018,0x8000310f,0x2df3b018,0xb0d02a41,0x8000b10f,0xaf282f28,0x310f8000,0xaf28af28,
0x2df3b018,0xb10f0000,0x2df33018,0x2a41b0d0,0x30d0aa41,0xaf28af28,0xaa4130d0,0x30ffa655,0x30d02a41,0x8000b10f,0xb0d02a41,0x2df33018,
0x2df33018,0x2f28af28,0xb018adf3,0xac983082,0x8000310f,0x310f8000,0x8000b10f,0xb10f0000,0xadf33018,0x2f282f28,0x3018adf3,0xac98b082,
0xb0d02a41,0x8000310f,0x30d02a41,0x2df3b018,0xb0d0aa41,0xaf282f28,0x2a4130d0,0x30ff2655,0xadf3b018,0xb10f0000,0xadf33018,0x2a4130d0,
0x8000b10f,0xaf28af28,0xb10f0000,0xaf282f28,0x2df3b018,0x8000b10f,0xadf3b018,0xb0d0aa41,0x30d0aa41,0x2f28af28,0x2a41b0d0,0xa655b0ff,
0x30d02a41,0x310f8000,0x30d0aa41,0x3018adf3,0x2df33018,0x2f282f28,0x30182df3,0x30822c98,0x30d02a41,0xadf3b018,0x8000310f,0x2df3b018,
0x8000310f,0x2f28af28,0xb10f0000,0x2f282f28,0xb0d02a41,0x30182df3,0x8000b10f,0xb0182df3,0xadf3b018,0xac983082,0x310f8000,0xac98b082,
0x2df3b018,0xb0d0aa41,0x8000310f,0x30d0aa41,0x30d02a41,0x2655b0ff,0xb10f0000,0x265530ff,0x8000310f,0x310f8000,0x8000b10f,0xb10f0000,
0xb0d02a41,0x265530ff,0x310f8000,0x2655b0ff,0xadf3b018,0xb0d02a41,0x8000310f,0x30d02a41,0x2df3b018,0xac98b082,0xb10f0000,0xac983082,
0x30d02a41,0x3018adf3,0x8000b10f,0xb018adf3,0x8000310f,0x2f282f28,0x310f8000,0x2f28af28,0xb0d02a41,0xadf33018,0x8000310f,0x2df33018,
0xadf3b018,0xb082ac98,0xb10f0000,0xb0822c98,0x2df3b018,0x2a41b0d0,0x8000b10f,0xaa41b0d0,0x30d02a41,0x30ff2655,0x310f8000,0x30ffa655,
0xb0d02a41,0x30d02a41,0xadf3b018,0x8000310f,0x8000b10f,0xaf282f28,0x310f8000,0xaf28af28,0x30d02a41,0xaa41b0d0,0xadf33018,0x310f8000,
0xadf33018,0x30ff2655,0xaa41b0d0,0xaf282f28,0xadf3b018,0xadf33018,0x30d02a41,0x8000b10f,0x30d0aa41,0xac98b082,0xb0182df3,0x2f282f28,
0x8000310f,0x310f8000,0x8000b10f,0xb10f0000,0xb0d0aa41,0xac983082,0x30182df3,0x2f28af28,0x2df3b018,0xadf3b018,0xb0d02a41,0x8000310f,
0x2df33018,0x30ffa655,0x2a41b0d0,0xaf28af28,0xb0d02a41,0xaa4130d0,0x2df33018,0x310f8000,0x8000b10f,0xaf28af28,0xb10f0000,0xaf282f28,
0x30d02a41,0x30d0aa41,0x2df3b018,0x8000b10f,0xadf33018,0xa65530ff,0x2a4130d0,0x2f282f28,0xadf3b018,0xb018adf3,0xb0d0aa41,0xb10f0000,
0x30d0aa41,0x3082ac98,0x3018adf3,0x2f28af28,0x310f0000,0xb018adf3,0x2a4130d0,0x2a41b0d0,0x310f0000,0xaf28af28,0x310f,0x2f28af28,
0x310f0000,0xadf3b018,0xaa4130d0,0x30d0aa41,0x310f0000,0xac98b082,0xadf33018,0x30ff2655,0x310f0000,0xaa41b0d0,0xb0182df3,0x30182df3,
0x310f0000,0xa655b0ff,0xb0d02a41,0x2c983082,0x310f0000,0x8000b10f,0xb10f0000,0x310f,0x310f0000,0x2655b0ff,0xb0d0aa41,0xac983082,
0x310f0000,0x2a41b0d0,0xb018adf3,0xb0182df3,0x310f0000,0x2c98b082,0xadf3b018,0xb0ff2655,0x310f0000,0x2df3b018,0xaa41b0d0,0xb0d0aa41,
0x310f0000,0x2f28af28,0x8000b10f,0xaf28af28,0x310f0000,0x3018adf3,0x2a41b0d0,0xaa41b0d0,0x310f0000,0x3082ac98,0x2df3b018,0x2655b0ff,
0x310f0000,0x30d0aa41,0x3018adf3,0x2df3b018,0x310f0000,0x30ffa655,0x30d0aa41,0x3082ac98,0xb0182df3,0x310f0000,0xb018adf3,0x2a4130d0,
0xb10f0000,0x2f282f28,0x8000b10f,0xaf282f28,0xb018adf3,0x310f,0x3018adf3,0xb0d0aa41,0xaa41b0d0,0xaf282f28,0x30d02a41,0xa655b0ff,
0x2a41b0d0,0xb10f0000,0x2a4130d0,0x3018adf3,0x3018adf3,0xaf28af28,0xadf33018,0x30822c98,0x310f0000,0x8000b10f,0xb10f0000,0x310f,
0x30182df3,0x2f28af28,0xadf3b018,0xb0822c98,0x2a4130d0,0x310f0000,0x2a41b0d0,0xb018adf3,0xaa4130d0,0x2f282f28,0x30d0aa41,0x2655b0ff,
0xb0182df3,0x310f,0x30182df3,0x30d0aa41,0xb10f0000,0xaf282f28,0x310f,0x2f282f28,0xb018adf3,0xb10f0000,0xb0182df3,0xaa4130d0,
0xaa41b0d0,0xaf28af28,0xb0d0aa41,0xb0ff2655,0x2a41b0d0,0x8000b10f,0xaa41b0d0,0xadf3b018,0x3018adf3,0x2f28af28,0x2df3b018,0x2c98b082,
0x2a41b0d0,0xb0182df3,0x310f0000,0xb018adf3,0x310f0000,0xaf28af28,0x310f,0x2f28af28,0x2a4130d0,0x2df3b018,0xb10f0000,0x2df33018,
0xb0182df3,0x30822c98,0x8000b10f,0xb0822c98,0xb018adf3,0xaa4130d0,0x310f0000,0xaa41b0d0,0x2a41b0d0,0xb0ffa655,0x310f,0x30ffa655,
0x310f0000,0x8000b10f,0xb10f0000,0x310f,0x2a4130d0,0x30ffa655,0x8000b10f,0xb0ffa655,0xb0182df3,0x2a4130d0,0x310f0000,0x2a41b0d0,
0xb018adf3,0xb0822c98,0x310f,0x30822c98,0x2a41b0d0,0xadf3b018,0xb10f0000,0xadf33018,0x310f0000,0x2f28af28,0x8000b10f,0xaf28af28,
0x2a4130d0,0x30182df3,0x310f0000,0x3018adf3,0xb0182df3,0xac983082,0x310f,0x2c983082,0xb018adf3,0xb0d0aa41,0xb10f0000,0xb0d02a41,
0x2a41b0d0,0x2655b0ff,0x8000b10f,0xa655b0ff,0x2a4130d0,0x2a41b0d0,0xb0182df3,0x310f0000,0xb10f0000,0x2f282f28,0x8000b10f,0xaf282f28,
0x2a41b0d0,0xb0d02a41,0x30182df3,0x8000b10f,0x30182df3,0x2655b0ff,0xb0d02a41,0x2f282f28,0xb0182df3,0x30182df3,0x2a41b0d0,0xb10f0000,
0xaa41b0d0,0xb0822c98,0x2df33018,0x2f28af28,0x310f0000,0x8000b10f,0xb10f0000,0x310f,0xaa4130d0,0x30822c98,0x2df3b018,0xaf28af28,
0xb018adf3,0xb0182df3,0x2a4130d0,0x310f0000,0x3018adf3,0xa655b0ff,0xb0d0aa41,0xaf282f28,0x2a4130d0,0x30d02a41,0x3018adf3,0x8000b10f,
0xb10f0000,0xaf282f28,0x310f,0x2f282f28,0x2a41b0d0,0xaa41b0d0,0xb018adf3,0xb10f0000,0x30182df3,0x30ff2655,0x30d0aa41,0x2f28af28,
0xb0182df3,0xadf33018,0xaa4130d0,0x310f,0xaa41b0d0,0xac98b082,0xadf3b018,0xaf28af28,0x2df3b018,0xb0d02a41,0x30d02a41,0xadf3b018,
0x8000310f,0x2f28af28,0xb10f0000,0x2f282f28,0xadf3b018,0xaa4130d0,0x30d0aa41,0xb018adf3,0x30d02a41,0xa655b0ff,0xb0182df3,0x30822c98,
0xb0d02a41,0x2df33018,0x2df3b018,0xb0d0aa41,0x2df3b018,0xb082ac98,0xaa4130d0,0x30ff2655,0x8000310f,0x310f8000,0x8000b10f,0xb10f0000,
0xadf3b018,0xb0822c98,0x2a4130d0,0x30ffa655,0x30d02a41,0x2df3b018,0xadf3b018,0xb0d02a41,0xb0d02a41,0xa65530ff,0x30182df3,0x3082ac98,
0x2df3b018,0xaa41b0d0,0xb0d0aa41,0xb0182df3,0x8000310f,0x2f282f28,0x310f8000,0x2f28af28,0xadf3b018,0xb0d0aa41,0xb0d02a41,0xadf33018,
0x30d02a41,0x30ffa655,0x3018adf3,0x2c98b082,0xb0d02a41,0xb0182df3,0xadf33018,0xaa4130d0,0x2df3b018,0x2c98b082,0x2a41b0d0,0x2655b0ff,
0x8000310f,0x2df3b018,0xb0d02a41,0x30d02a41,0x8000b10f,0xaf282f28,0x310f8000,0xaf28af28,0x8000310f,0x3018adf3,0xb0d0aa41,0x2a4130d0,
0x8000b10f,0xb0822c98,0x30182df3,0x2655b0ff,0x8000310f,0x30d0aa41,0xadf3b018,0xadf33018,0x8000b10f,0xb0ff2655,0x2a4130d0,0x3082ac98,
0x8000310f,0x310f8000,0x8000b10f,0xb10f0000,0x8000b10f,0xb0ffa655,0xaa4130d0,0x30822c98,0x8000310f,0x30d02a41,0x2df3b018,0xadf3b018,
0x8000b10f,0xb082ac98,0xb0182df3,0x265530ff,0x8000310f,0x30182df3,0x30d0aa41,0x2a41b0d0,0x8000b10f,0xaf28af28,0xb10f0000,0xaf282f28,
0x8000310f,0x2df33018,0x30d02a41,0x30d0aa41,0x8000b10f,0xac98b082,0xb018adf3,0xb0ffa655,0x8000310f,0x2a4130d0,0x2df33018,0x30182df3,
0x8000b10f,0xa655b0ff,0xaa41b0d0,0xac98b082,0xadf3b018,0x8000310f,0x2df3b018,0xb0d02a41,0x8000310f,0x2f28af28,0xb10f0000,0x2f282f28,
0x2df3b018,0xb10f0000,0x2df33018,0x2a41b0d0,0xb0d02a41,0x2f282f28,0x2a41b0d0,0xb0ff2655,0x30d02a41,0x8000b10f,0xb0d02a41,0x2df33018,
0xadf3b018,0xaf282f28,0x30182df3,0x2c98b082,0x8000310f,0x310f8000,0x8000b10f,0xb10f0000,0x2df3b018,0xaf28af28,0xb0182df3,0x2c983082,
0xb0d02a41,0x8000310f,0x30d02a41,0x2df3b018,0x30d02a41,0x2f28af28,0xaa41b0d0,0xb0ffa655,0xadf3b018,0xb10f0000,0xadf33018,0x2a4130d0,
0x8000310f,0x2f282f28,0x310f8000,0x2f28af28,0x2df3b018,0x8000b10f,0xadf3b018,0xb0d0aa41,0xb0d02a41,0xaf282f28,0xaa4130d0,0x265530ff,
0x30d02a41,0x310f8000,0x30d0aa41,0x3018adf3,0xadf3b018,0xaf28af28,0xb018adf3,0xb082ac98,0x30d02a41,0xadf3b018,0x8000310f,0x2df3b018,
0x8000b10f,0xaf282f28,0x310f8000,0xaf28af28,0xb0d02a41,0x30182df3,0x8000b10f,0xb0182df3,0x2df33018,0x2c98b082,0xb10f0000,0x2c983082,
0x2df3b018,0xb0d0aa41,0x8000310f,0x30d0aa41,0xb0d0aa41,0xa65530ff,0x310f8000,0xa655b0ff,0x8000310f,0x310f8000,0x8000b10f,0xb10f0000,
0x30d0aa41,0xa655b0ff,0xb10f0000,0xa65530ff,0xadf3b018,0xb0d02a41,0x8000310f,0x30d02a41,0xadf33018,0x2c983082,0x310f8000,0x2c98b082,
0x30d02a41,0x3018adf3,0x8000b10f,0xb018adf3,0x8000b10f,0xaf28af28,0xb10f0000,0xaf282f28,0xb0d02a41,0xadf33018,0x8000310f,0x2df33018,
0x2df33018,0x30822c98,0x310f8000,0x3082ac98,0x2df3b018,0x2a41b0d0,0x8000b10f,0xaa41b0d0,0xb0d0aa41,0xb0ffa655,0xb10f0000,0xb0ff2655,
0xb018adf3,0x2a4130d0,0x2a41b0d0,0xb0182df3,0x310f0000,0xaf28af28,0x310f,0x2f28af28,0xb0182df3,0x30d02a41,0xaa41b0d0,0xadf33018,
0x2a41b0d0,0xb0ff2655,0x2df33018,0x2c98b082,0x2a4130d0,0x3018adf3,0xb018adf3,0xaa4130d0,0xb018adf3,0xac983082,0x30d02a41,0x2655b0ff,
0x310f0000,0x8000b10f,0xb10f0000,0x310f,0xb0182df3,0x2c983082,0x30d0aa41,0xa655b0ff,0x2a41b0d0,0xb018adf3,0xb0182df3,0x2a4130d0,
0x2a4130d0,0x30ff2655,0x2df3b018,0xac98b082,0xb018adf3,0xb0d02a41,0xaa4130d0,0x2df33018,0x310f0000,0x2f28af28,0x8000b10f,0xaf28af28,
0xb0182df3,0xaa4130d0,0x2a4130d0,0x30182df3,0x2a41b0d0,0xa655b0ff,0xadf3b018,0xb082ac98,0x2a4130d0,0x2df33018,0x30182df3,0x30d02a41,
0xb018adf3,0xb082ac98,0xb0d0aa41,0xb0ffa655,0x310f0000,0xb018adf3,0x2a4130d0,0x2a41b0d0,0xb10f0000,0x2f282f28,0x8000b10f,0xaf282f28,
0x310f0000,0xadf3b018,0xaa4130d0,0x30d0aa41,0xb10f0000,0x2c983082,0x2df3b018,0xb0ffa655,0x310f0000,0xaa41b0d0,0xb0182df3,0x30182df3,
0xb10f0000,0x265530ff,0x30d0aa41,0xac98b082,0x310f0000,0x8000b10f,0xb10f0000,0x310f,0xb10f0000,0xa65530ff,0x30d02a41,0x2c98b082,
0x310f0000,0x2a41b0d0,0xb018adf3,0xb0182df3,0xb10f0000,0xac983082,0x2df33018,0x30ffa655,0x310f0000,0x2df3b018,0xaa41b0d0,0xb0d0aa41,
0xb10f0000,0xaf282f28,0x310f,0x2f282f28,0x310f0000,0x3018adf3,0x2a41b0d0,0xaa41b0d0,0xb10f0000,0xb0822c98,0xadf33018,0xa65530ff,
0x310f0000,0x30d0aa41,0x3018adf3,0x2df3b018,0xb10f0000,0xb0ff2655,0xb0d02a41,0xb0822c98,0xb0182df3,0x310f0000,0xb018adf3,0x2a4130d0,
0x310f0000,0xaf28af28,0x310f,0x2f28af28,0xb018adf3,0x310f,0x3018adf3,0xb0d0aa41,0x2a4130d0,0x2f28af28,0xb0d0aa41,0x265530ff,
0x2a41b0d0,0xb10f0000,0x2a4130d0,0x3018adf3,0xb0182df3,0x2f282f28,0x2df3b018,0xb082ac98,0x310f0000,0x8000b10f,0xb10f0000,0x310f,
0xb018adf3,0xaf282f28,0x2df33018,0x3082ac98,0x2a4130d0,0x310f0000,0x2a41b0d0,0xb018adf3,0x2a41b0d0,0xaf28af28,0xb0d02a41,0xa65530ff,
0xb0182df3,0x310f,0x30182df3,0x30d0aa41,0x310f0000,0x2f28af28,0x8000b10f,0xaf28af28,0xb018adf3,0xb10f0000,0xb0182df3,0xaa4130d0,
0x2a4130d0,0x2f282f28,0x30d02a41,0x30ffa655,0x2a41b0d0,0x8000b10f,0xaa41b0d0,0xadf3b018,0xb0182df3,0xaf282f28,0xadf33018,0xac983082,
0x2a41b0d0,0xb0182df3,0x310f0000,0xb018adf3,0xb10f0000,0x2f282f28,0x8000b10f,0xaf282f28,0x2a4130d0,0x2df3b018,0xb10f0000,0x2df33018,
0x3018adf3,0xb082ac98,0x310f,0x3082ac98,0xb018adf3,0xaa4130d0,0x310f0000,0xaa41b0d0,0xaa4130d0,0x30ff2655,0x8000b10f,0xb0ff2655,
0x310f0000,0x8000b10f,0xb10f0000,0x310f,0xaa41b0d0,0xb0ff2655,0x310f,0x30ff2655,0xb0182df3,0x2a4130d0,0x310f0000,0x2a41b0d0,
0x30182df3,0x3082ac98,0x8000b10f,0xb082ac98,0x2a41b0d0,0xadf3b018,0xb10f0000,0xadf33018,0xb10f0000,0xaf282f28,0x310f,0x2f282f28,
0x2a4130d0,0x30182df3,0x310f0000,0x3018adf3,0x3018adf3,0x2c98b082,0x8000b10f,0xac98b082,0xb018adf3,0xb0d0aa41,0xb10f0000,0xb0d02a41,
0xaa4130d0,0xa65530ff,0x310f,0x265530ff,0xb0d02a41,0x30d02a41,0xadf3b018,0x8000310f,0x8000310f,0x2f28af28,0xb10f0000,0x2f282f28,
0x30d02a41,0xaa41b0d0,0xadf33018,0x310f8000,0x2df3b018,0xb0ffa655,0x2a4130d0,0x2f28af28,0xadf3b018,0xadf33018,0x30d02a41,0x8000b10f,
0xb0d02a41,0x2c983082,0x3018adf3,0xaf28af28,0x8000310f,0x310f8000,0x8000b10f,0xb10f0000,0x30d02a41,0x2c98b082,0xb018adf3,0xaf282f28,
0x2df3b018,0xadf3b018,0xb0d02a41,0x8000310f,0xadf3b018,0xb0ff2655,0xaa4130d0,0x2f282f28,0xb0d02a41,0xaa4130d0,0x2df33018,0x310f8000,
0x8000310f,0x2f282f28,0x310f8000,0x2f28af28,0x30d02a41,0x30d0aa41,0x2df3b018,0x8000b10f,0x2df3b018,0x2655b0ff,0xaa41b0d0,0xaf28af28,
0xadf3b018,0xb018adf3,0xb0d0aa41,0xb10f0000,0xb0d02a41,0xb0822c98,0xb0182df3,0xaf282f28,0x2df3b018,0xb0d02a41,0x30d02a41,0xadf3b018,
0x8000b10f,0xaf282f28,0x310f8000,0xaf28af28,0xadf3b018,0xaa4130d0,0x30d0aa41,0xb018adf3,0xb0d0aa41,0x265530ff,0x3018adf3,0xb082ac98,
0xb0d02a41,0x2df33018,0x2df3b018,0xb0d0aa41,0xadf33018,0x30822c98,0x2a41b0d0,0xb0ffa655,0x8000310f,0x310f8000,0x8000b10f,0xb10f0000,
0x2df33018,0x3082ac98,0xaa41b0d0,0xb0ff2655,0x30d02a41,0x2df3b018,0xadf3b018,0xb0d02a41,0x30d0aa41,0x2655b0ff,0xb018adf3,0xb0822c98,
0x2df3b018,0xaa41b0d0,0xb0d0aa41,0xb0182df3,0x8000b10f,0xaf28af28,0xb10f0000,0xaf282f28,0xadf3b018,0xb0d0aa41,0xb0d02a41,0xadf33018,
0xb0d0aa41,0xb0ff2655,0xb0182df3,0xac983082,0xb0d02a41,0xb0182df3,0xadf33018,0xaa4130d0,0xadf33018,0xac983082,0xaa4130d0,0xa65530ff,
0x0,0x0,0x0,0x0,0x0,0x0,0x0,0x0,0x0,0x0,0x0,0x0,
0x0,0x0,0x0,0x0,0x0,0x0,0x0,0x0,0x0,0x0,0x0,0x0,
0x0,0x0,0x0,0x0,0x0,0x0,0x0,0x0,0x0,0x0,0x0,0x0,
0x0,0x0,0x0,0x0,0x0,0x0,0x0,0x0,0x0,0x0,0x0,0x0,
0x0,0x0,0x0,0x0,0x0,0x0,0x0,0x0,0x0,0x0,0x0,0x0,
0x0,0x0,0x0,0x0,0x0,0x0,0x0,0x0,0x0,0x0,0x0,0x0,
0x0,0x0,0x0,0x0,0x0,0x0,0x0,0x0,0x0,0x0,0x0,0x0,
0x0,0x0,0x0,0x0,0x0,0x0,0x0,0x0,0x0,0x0,0x0,0x0,
0x0,0x0,0x0,0x0,0x0,0x0,0x0,0x0,0x0,0x0,0x0,0x0,
0x0,0x0,0x0,0x0,0x0,0x0,0x0,0x0,0x0,0x0,0x0,0x0,
0x0,0x0,0x0,0x0,0x0,0x0,0x0,0x0,0x2a4130d0,0x2a41b0d0,0xb0182df3,0x310f0000,
0x310f0000,0xaf28af28,0x310f,0x2f28af28,0x2a41b0d0,0xb0d02a41,0x30182df3,0x8000b10f,0xb018adf3,0xa65530ff,0x30d0aa41,0xaf28af28,
0xb0182df3,0x30182df3,0x2a41b0d0,0xb10f0000,0x2a4130d0,0x3082ac98,0xadf3b018,0xaf282f28,0x310f0000,0x8000b10f,0xb10f0000,0x310f,
0x2a41b0d0,0xb082ac98,0xadf33018,0x2f282f28,0xb018adf3,0xb0182df3,0x2a4130d0,0x310f0000,0xb0182df3,0x265530ff,0x30d02a41,0x2f28af28,
0x2a4130d0,0x30d02a41,0x3018adf3,0x8000b10f,0x310f0000,0x2f28af28,0x8000b10f,0xaf28af28,0x2a41b0d0,0xaa41b0d0,0xb018adf3,0xb10f0000,
0xb018adf3,0xb0ffa655,0xb0d02a41,0xaf282f28,0xb0182df3,0xadf33018,0xaa4130d0,0x310f,0x2a4130d0,0x2c983082,0x2df33018,0x2f282f28,
0xb018adf3,0x2a4130d0,0x2a41b0d0,0xb0182df3,0xb10f0000,0x2f282f28,0x8000b10f,0xaf282f28,0xb0182df3,0x30d02a41,0xaa41b0d0,0xadf33018,
0xaa4130d0,0x30ffa655,0xadf3b018,0xac983082,0x2a4130d0,0x3018adf3,0xb018adf3,0xaa4130d0,0x30182df3,0x2c98b082,0xb0d0aa41,0xa65530ff,
0x310f0000,0x8000b10f,0xb10f0000,0x310f,0x3018adf3,0xac98b082,0xb0d02a41,0x265530ff,0x2a41b0d0,0xb018adf3,0xb0182df3,0x2a4130d0,
0xaa41b0d0,0xb0ffa655,0xadf33018,0x2c983082,0xb018adf3,0xb0d02a41,0xaa4130d0,0x2df33018,0xb10f0000,0xaf282f28,0x310f,0x2f282f28,
0xb0182df3,0xaa4130d0,0x2a4130d0,0x30182df3,0xaa4130d0,0x265530ff,0x2df33018,0x30822c98,0x2a4130d0,0x2df33018,0x30182df3,0x30d02a41,
0x30182df3,0x30822c98,0x30d02a41,0x30ff2655,0x0,0x0,0x0,0x0,0x0,0x0,0x0,0x0,
0x0,0x0,0x0,0x0,0x0,0x0,0x0,0x0,0x0,0x0,0x0,0x0,
0x0,0x0,0x0,0x0,0x0,0x0,0x0,0x0,0x0,0x0,0x0,0x0,
0x0,0x0,0x0,0x0,0x0,0x0,0x0,0x0,0x0,0x0,0x0,0x0,
0x0,0x0,0x0,0x0,0x0,0x0,0x0,0x0,0x0,0x0,0x0,0x0,
0x0,0x0,0x0,0x0,0x0,0x0,0x0,0x0,0x0,0x0,0x0,0x0,
0x0,0x0,0x0,0x0,0x0,0x0,0x0,0x0,0x0,0x0,0x0,0x0,
0x0,0x0,0x0,0x0,0x0,0x0,0x0,0x0,0x0,0x0,0x0,0x0,
0x0,0x0,0x0,0x0,0x0,0x0,0x0,0x0,0x0,0x0,0x0,0x0,
0x0,0x0,0x0,0x0,0x0,0x0,0x0,0x0,0x0,0x0,0x0,0x0,
0x0,0x0,0x0,0x0,0x0,0x0,0x0,0x0,0x0,0x0,0x0,0x0,
};
__device__ __attribute__((aligned(16))) const unsigned int RQ_TAB[2048] = {
0x30003000,0x30003000,0x30003000,0x30003000,0x2f643000,0x2a1f2da8,0xaa1f0000,0xaf64ada8,0x2da83000,0xada80000,0xada8b000,0x2da88000,
0x2a1f3000,0xaf64ada8,0x2f648000,0xaa1f2da8,0x3000,0x8000b000,0x3000,0x8000b000,0xaa1f3000,0x2f64ada8,0xaf640000,0x2a1f2da8,
0xada83000,0x2da88000,0x2da8b000,0xada80000,0xaf643000,0xaa1f2da8,0x2a1f8000,0x2f64ada8,0xb0003000,0xb0003000,0xb0003000,0xb0003000,
0xaf643000,0xaa1f2da8,0x2a1f0000,0x2f64ada8,0xada83000,0x2da80000,0x2da8b000,0xada88000,0xaa1f3000,0x2f64ada8,0xaf648000,0x2a1f2da8,
0x80003000,0xb000,0x80003000,0xb000,0x2a1f3000,0xaf64ada8,0x2f640000,0xaa1f2da8,0x2da83000,0xada88000,0xada8b000,0x2da80000,
0x2f643000,0x2a1f2da8,0xaa1f8000,0xaf64ada8,0x30003000,0x30003000,0x30003000,0x30003000,0xaf64b000,0xaa1fada8,0x2a1f8000,0x2f642da8,
0x2da83000,0xada80000,0xada8b000,0x2da88000,0xaa1fb000,0x2f642da8,0xaf640000,0x2a1fada8,0x3000,0x8000b000,0x3000,0x8000b000,
0x2a1fb000,0xaf642da8,0x2f648000,0xaa1fada8,0xada83000,0x2da88000,0x2da8b000,0xada80000,0x2f64b000,0x2a1fada8,0xaa1f0000,0xaf642da8,
0xb0003000,0xb0003000,0xb0003000,0xb0003000,0x2f64b000,0x2a1fada8,0xaa1f8000,0xaf642da8,0xada83000,0x2da80000,0x2da8b000,0xada88000,
0x2a1fb000,0xaf642da8,0x2f640000,0xaa1fada8,0x80003000,0xb000,0x80003000,0xb000,0xaa1fb000,0x2f642da8,0xaf648000,0x2a1fada8,
0x2da83000,0xada88000,0xada8b000,0x2da80000,0xaf64b000,0xaa1fada8,0x2a1f0000,0x2f642da8,0x80008000,0x80008000,0x80008000,0x80008000,
0x2a1f8000,0x2f642da8,0x2f643000,0x2a1f2da8,0x2da88000,0x2da83000,0xada80000,0xada8b000,0x2f648000,0xaa1f2da8,0xaa1fb000,0x2f642da8,
0x30008000,0xb0000000,0x30008000,0xb0000000,0x2f648000,0xaa1fada8,0xaa1f3000,0x2f64ada8,0x2da88000,0x2da8b000,0xada80000,0xada83000,
0x2a1f8000,0x2f64ada8,0x2f64b000,0x2a1fada8,0x8000,0x8000,0x8000,0x8000,0xaa1f8000,0xaf642da8,0xaf643000,0xaa1f2da8,
0xada88000,0xada83000,0x2da80000,0x2da8b000,0xaf648000,0x2a1f2da8,0x2a1fb000,0xaf642da8,0xb0008000,0x30000000,0xb0008000,0x30000000,
0xaf648000,0x2a1fada8,0x2a1f3000,0xaf64ada8,0xada88000,0xada8b000,0x2da80000,0x2da83000,0xaa1f8000,0xaf64ada8,0xaf64b000,0xaa1fada8,
0x80008000,0x80008000,0x80008000,0x80008000,0xaa1f0000,0xaf64ada8,0xaf64b000,0xaa1fada8,0x2da88000,0x2da83000,0xada80000,0xada8b000,
0xaf640000,0x2a1fada8,0x2a1f3000,0xaf64ada8,0x30008000,0xb0000000,0x30008000,0xb0000000,0xaf640000,0x2a1f2da8,0x2a1fb000,0xaf642da8,
0x2da88000,0x2da8b000,0xada80000,0xada83000,0xaa1f0000,0xaf642da8,0xaf643000,0xaa1f2da8,0x8000,0x8000,0x8000,0x8000,
0x2a1f0000,0x2f64ada8,0x2f64b000,0x2a1fada8,0xada88000,0xada83000,0x2da80000,0x2da8b000,0x2f640000,0xaa1fada8,0xaa1f3000,0x2f64ada8,
0xb0008000,0x30000000,0xb0008000,0x30000000,0x2f640000,0xaa1f2da8,0xaa1fb000,0x2f642da8,0xada88000,0xada8b000,0x2da80000,0x2da83000,
0x2a1f0000,0x2f642da8,0x2f643000,0x2a1f2da8,0x0,0x0,0x0,0x0,0xaa1f0000,0xaf64ada8,0xaf64b000,0xaa1fada8,
0xada80000,0xada8b000,0x2da88000,0x2da83000,0xaf640000,0x2a1fada8,0x2a1f3000,0xaf64ada8,0xb0000000,0x30008000,0xb0000000,0x30008000,
0xaf640000,0x2a1f2da8,0x2a1fb000,0xaf642da8,0xada80000,0xada83000,0x2da88000,0x2da8b000,0xaa1f0000,0xaf642da8,0xaf643000,0xaa1f2da8,
0x80000000,0x80000000,0x80000000,0x80000000,0x2a1f0000,0x2f64ada8,0x2f64b000,0x2a1fada8,0x2da80000,0x2da8b000,0xada88000,0xada83000,
0x2f640000,0xaa1fada8,0xaa1f3000,0x2f64ada8,0x30000000,0xb0008000,0x30000000,0xb0008000,0x2f640000,0xaa1f2da8,0xaa1fb000,0x2f642da8,
0x2da80000,0x2da83000,0xada88000,0xada8b000,0x2a1f0000,0x2f642da8,0x2f643000,0x2a1f2da8,0x0,0x0,0x0,0x0,
0x2a1f8000,0x2f642da8,0x2f643000,0x2a1f2da8,0xada80000,0xada8b000,0x2da88000,0x2da83000,0x2f648000,0xaa1f2da8,0xaa1fb000,0x2f642da8,
0xb0000000,0x30008000,0xb0000000,0x30008000,0x2f648000,0xaa1fada8,0xaa1f3000,0x2f64ada8,0xada80000,0xada83000,0x2da88000,0x2da8b000,
0x2a1f8000,0x2f64ada8,0x2f64b000,0x2a1fada8,0x80000000,0x80000000,0x80000000,0x80000000,0xaa1f8000,0xaf642da8,0xaf643000,0xaa1f2da8,
0x2da80000,0x2da8b000,0xada88000,0xada83000,0xaf648000,0x2a1f2da8,0x2a1fb000,0xaf642da8,0x30000000,0xb0008000,0x30000000,0xb0008000,
0xaf648000,0x2a1fada8,0x2a1f3000,0xaf64ada8,0x2da80000,0x2da83000,0xada88000,0xada8b000,0xaa1f8000,0xaf64ada8,0xaf64b000,0xaa1fada8,
0x30003000,0x30003000,0x30003000,0x30003000,0x2f643000,0x2a1f2da8,0xaa1f0000,0xaf64ada8,0x2da83000,0xada80000,0xada8b000,0x2da88000,
0x2a1f3000,0xaf64ada8,0x2f648000,0xaa1f2da8,0x3000,0x8000b000,0x3000,0x8000b000,0xaa1f3000,0x2f64ada8,0xaf640000,0x2a1f2da8,
0xada83000,0x2da88000,0x2da8b000,0xada80000,0xaf643000,0xaa1f2da8,0x2a1f8000,0x2f64ada8,0xb0003000,0xb0003000,0xb0003000,0xb0003000,
0xaf643000,0xaa1f2da8,0x2a1f0000,0x2f64ada8,0xada83000,0x2da80000,0x2da8b000,0xada88000,0xaa1f3000,0x2f64ada8,0xaf648000,0x2a1f2da8,
0x80003000,0xb000,0x80003000,0xb000,0x2a1f3000,0xaf64ada8,0x2f640000,0xaa1f2da8,0x2da83000,0xada88000,0xada8b000,0x2da80000,
0x2f643000,0x2a1f2da8,0xaa1f8000,0xaf64ada8,0x30003000,0x30003000,0x30003000,0x30003000,0xaf64b000,0xaa1fada8,0x2a1f8000,0x2f642da8,
0x2da83000,0xada80000,0xada8b000,0x2da88000,0xaa1fb000,0x2f642da8,0xaf640000,0x2a1fada8,0x3000,0x8000b000,0x3000,0x8000b000,
0x2a1fb000,0xaf642da8,0x2f648000,0xaa1fada8,0xada83000,0x2da88000,0x2da8b000,0xada80000,0x2f64b000,0x2a1fada8,0xaa1f0000,0xaf642da8,
0xb0003000,0xb0003000,0xb0003000,0xb0003000,0x2f64b000,0x2a1fada8,0xaa1f8000,0xaf642da8,0xada83000,0x2da80000,0x2da8b000,0xada88000,
0x2a1fb000,0xaf642da8,0x2f640000,0xaa1fada8,0x80003000,0xb000,0x80003000,0xb000,0xaa1fb000,0x2f642da8,0xaf648000,0x2a1fada8,
0x2da83000,0xada88000,0xada8b000,0x2da80000,0xaf64b000,0xaa1fada8,0x2a1f0000,0x2f642da8,0xb000b000,0xb000b000,0xb000b000,0xb000b000,
0xaf64b000,0xaa1fada8,0x2a1f8000,0x2f642da8,0xada8b000,0x2da88000,0x2da83000,0xada80000,0xaa1fb000,0x2f642da8,0xaf640000,0x2a1fada8,
0x8000b000,0x3000,0x8000b000,0x3000,0x2a1fb000,0xaf642da8,0x2f648000,0xaa1fada8,0x2da8b000,0xada80000,0xada83000,0x2da88000,
0x2f64b000,0x2a1fada8,0xaa1f0000,0xaf642da8,0x3000b000,0x3000b000,0x3000b000,0x3000b000,0x2f64b000,0x2a1fada8,0xaa1f8000,0xaf642da8,
0x2da8b000,0xada88000,0xada83000,0x2da80000,0x2a1fb000,0xaf642da8,0x2f640000,0xaa1fada8,0xb000,0x80003000,0xb000,0x80003000,
0xaa1fb000,0x2f642da8,0xaf648000,0x2a1fada8,0xada8b000,0x2da80000,0x2da83000,0xada88000,0xaf64b000,0xaa1fada8,0x2a1f0000,0x2f642da8,
0xb000b000,0xb000b000,0xb000b000,0xb000b000,0x2f643000,0x2a1f2da8,0xaa1f0000,0xaf64ada8,0xada8b000,0x2da88000,0x2da83000,0xada80000,
0x2a1f3000,0xaf64ada8,0x2f648000,0xaa1f2da8,0x8000b000,0x3000,0x8000b000,0x3000,0xaa1f3000,0x2f64ada8,0xaf640000,0x2a1f2da8,
0x2da8b000,0xada80000,0xada83000,0x2da88000,0xaf643000,0xaa1f2da8,0x2a1f8000,0x2f64ada8,0x3000b000,0x3000b000,0x3000b000,0x3000b000,
0xaf643000,0xaa1f2da8,0x2a1f0000,0x2f64ada8,0x2da8b000,0xada88000,0xada83000,0x2da80000,0xaa1f3000,0x2f64ada8,0xaf648000,0x2a1f2da8,
0xb000,0x80003000,0xb000,0x80003000,0x2a1f3000,0xaf64ada8,0x2f640000,0xaa1f2da8,0xada8b000,0x2da80000,0x2da83000,0xada88000,
0x2f643000,0x2a1f2da8,0xaa1f8000,0xaf64ada8,0x0,0x0,0x0,0x0,0xaa1f0000,0xaf64ada8,0xaf64b000,0xaa1fada8,
0xada80000,0xada8b000,0x2da88000,0x2da83000,0xaf640000,0x2a1fada8,0x2a1f3000,0xaf64ada8,0xb0000000,0x30008000,0xb0000000,0x30008000,
0xaf640000,0x2a1f2da8,0x2a1fb000,0xaf642da8,0xada80000,0xada83000,0x2da88000,0x2da8b000,0xaa1f0000,0xaf642da8,0xaf643000,0xaa1f2da8,
0x80000000,0x80000000,0x80000000,0x80000000,0x2a1f0000,0x2f64ada8,0x2f64b000,0x2a1fada8,0x2da80000,0x2da8b000,0xada88000,0xada83000,
0x2f640000,0xaa1fada8,0xaa1f3000,0x2f64ada8,0x30000000,0xb0008000,0x30000000,0xb0008000,0x2f640000,0xaa1f2da8,0xaa1fb000,0x2f642da8,
0x2da80000,0x2da83000,0xada88000,0xada8b000,0x2a1f0000,0x2f642da8,0x2f643000,0x2a1f2da8,0x0,0x0,0x0,0x0,
0x2a1f8000,0x2f642da8,0x2f643000,0x2a1f2da8,0xada80000,0xada8b000,0x2da88000,0x2da83000,0x2f648000,0xaa1f2da8,0xaa1fb000,0x2f642da8,
0xb0000000,0x30008000,0xb0000000,0x30008000,0x2f648000,0xaa1fada8,0xaa1f3000,0x2f64ada8,0xada80000,0xada83000,0x2da88000,0x2da8b000,
0x2a1f8000,0x2f64ada8,0x2f64b000,0x2a1fada8,0x80000000,0x80000000,0x80000000,0x80000000,0xaa1f8000,0xaf642da8,0xaf643000,0xaa1f2da8,
0x2da80000,0x2da8b000,0xada88000,0xada83000,0xaf648000,0x2a1f2da8,0x2a1fb000,0xaf642da8,0x30000000,0xb0008000,0x30000000,0xb0008000,
0xaf648000,0x2a1fada8,0x2a1f3000,0xaf64ada8,0x2da80000,0x2da83000,0xada88000,0xada8b000,0xaa1f8000,0xaf64ada8,0xaf64b000,0xaa1fada8,
0x80008000,0x80008000,0x80008000,0x80008000,0x2a1f8000,0x2f642da8,0x2f643000,0x2a1f2da8,0x2da88000,0x2da83000,0xada80000,0xada8b000,
0x2f648000,0xaa1f2da8,0xaa1fb000,0x2f642da8,0x30008000,0xb0000000,0x30008000,0xb0000000,0x2f648000,0xaa1fada8,0xaa1f3000,0x2f64ada8,
0x2da88000,0x2da8b000,0xada80000,0xada83000,0x2a1f8000,0x2f64ada8,0x2f64b000,0x2a1fada8,0x8000,0x8000,0x8000,0x8000,
0xaa1f8000,0xaf642da8,0xaf643000,0xaa1f2da8,0xada88000,0xada83000,0x2da80000,0x2da8b000,0xaf648000,0x2a1f2da8,0x2a1fb000,0xaf642da8,
0xb0008000,0x30000000,0xb0008000,0x30000000,0xaf648000,0x2a1fada8,0x2a1f3000,0xaf64ada8,0xada88000,0xada8b000,0x2da80000,0x2da83000,
0xaa1f8000,0xaf64ada8,0xaf64b000,0xaa1fada8,0x80008000,0x80008000,0x80008000,0x80008000,0xaa1f0000,0xaf64ada8,0xaf64b000,0xaa1fada8,
0x2da88000,0x2da83000,0xada80000,0xada8b000,0xaf640000,0x2a1fada8,0x2a1f3000,0xaf64ada8,0x30008000,0xb0000000,0x30008000,0xb0000000,
0xaf640000,0x2a1f2da8,0x2a1fb000,0xaf642da8,0x2da88000,0x2da8b000,0xada80000,0xada83000,0xaa1f0000,0xaf642da8,0xaf643000,0xaa1f2da8,
0x8000,0x8000,0x8000,0x8000,0x2a1f0000,0x2f64ada8,0x2f64b000,0x2a1fada8,0xada88000,0xada83000,0x2da80000,0x2da8b000,
0x2f640000,0xaa1fada8,0xaa1f3000,0x2f64ada8,0xb0008000,0x30000000,0xb0008000,0x30000000,0x2f640000,0xaa1f2da8,0xaa1fb000,0x2f642da8,
0xada88000,0xada8b000,0x2da80000,0x2da83000,0x2a1f0000,0x2f642da8,0x2f643000,0x2a1f2da8,0xb000b000,0xb000b000,0xb000b000,0xb000b000,
0xaf64b000,0xaa1fada8,0x2a1f8000,0x2f642da8,0xada8b000,0x2da88000,0x2da83000,0xada80000,0xaa1fb000,0x2f642da8,0xaf640000,0x2a1fada8,
0x8000b000,0x3000,0x8000b000,0x3000,0x2a1fb000,0xaf642da8,0x2f648000,0xaa1fada8,0x2da8b000,0xada80000,0xada83000,0x2da88000,
0x2f64b000,0x2a1fada8,0xaa1f0000,0xaf642da8,0x3000b000,0x3000b000,0x3000b000,0x3000b000,0x2f64b000,0x2a1fada8,0xaa1f8000,0xaf642da8,
0x2da8b000,0xada88000,0xada83000,0x2da80000,0x2a1fb000,0xaf642da8,0x2f640000,0xaa1fada8,0xb000,0x80003000,0xb000,0x80003000,
0xaa1fb000,0x2f642da8,0xaf648000,0x2a1fada8,0xada8b000,0x2da80000,0x2da83000,0xada88000,0xaf64b000,0xaa1fada8,0x2a1f0000,0x2f642da8,
0xb000b000,0xb000b000,0xb000b000,0xb000b000,0x2f643000,0x2a1f2da8,0xaa1f0000,0xaf64ada8,0xada8b000,0x2da88000,0x2da83000,0xada80000,
0x2a1f3000,0xaf64ada8,0x2f648000,0xaa1f2da8,0x8000b000,0x3000,0x8000b000,0x3000,0xaa1f3000,0x2f64ada8,0xaf640000,0x2a1f2da8,
0x2da8b000,0xada80000,0xada83000,0x2da88000,0xaf643000,0xaa1f2da8,0x2a1f8000,0x2f64ada8,0x3000b000,0x3000b000,0x3000b000,0x3000b000,
0xaf643000,0xaa1f2da8,0x2a1f0000,0x2f64ada8,0x2da8b000,0xada88000,0xada83000,0x2da80000,0xaa1f3000,0x2f64ada8,0xaf648000,0x2a1f2da8,
0xb000,0x80003000,0xb000,0x80003000,0x2a1f3000,0xaf64ada8,0x2f640000,0xaa1f2da8,0xada8b000,0x2da80000,0x2da83000,0xada88000,
0x2f643000,0x2a1f2da8,0xaa1f8000,0xaf64ada8,0x30003000,0x30003000,0x0,0x0,0x2f643000,0x2a1f2da8,0xaa1f0000,0xaf64ada8,
0x2da83000,0xada80000,0xada80000,0xada8b000,0x2a1f3000,0xaf64ada8,0xaf640000,0x2a1fada8,0x3000,0x8000b000,0xb0000000,0x30008000,
0xaa1f3000,0x2f64ada8,0xaf640000,0x2a1f2da8,0xada83000,0x2da88000,0xada80000,0xada83000,0xaf643000,0xaa1f2da8,0xaa1f0000,0xaf642da8,
0xb0003000,0xb0003000,0x80000000,0x80000000,0xaf643000,0xaa1f2da8,0x2a1f0000,0x2f64ada8,0xada83000,0x2da80000,0x2da80000,0x2da8b000,
0xaa1f3000,0x2f64ada8,0x2f640000,0xaa1fada8,0x80003000,0xb000,0x30000000,0xb0008000,0x2a1f3000,0xaf64ada8,0x2f640000,0xaa1f2da8,
0x2da83000,0xada88000,0x2da80000,0x2da83000,0x2f643000,0x2a1f2da8,0x2a1f0000,0x2f642da8,0x30003000,0x30003000,0x0,0x0,
0xaa1f0000,0xaf64ada8,0xaf64b000,0xaa1fada8,0xada8b000,0x2da88000,0x2da88000,0x2da83000,0x2f648000,0xaa1f2da8,0x2a1f3000,0xaf64ada8,
0x3000,0x8000b000,0xb0000000,0x30008000,0xaf640000,0x2a1f2da8,0x2a1fb000,0xaf642da8,0x2da8b000,0xada80000,0x2da88000,0x2da8b000,
0x2a1f8000,0x2f64ada8,0xaf643000,0xaa1f2da8,0xb0003000,0xb0003000,0x80000000,0x80000000,0x2a1f0000,0x2f64ada8,0x2f64b000,0x2a1fada8,
0x2da8b000,0xada88000,0xada88000,0xada83000,0xaf648000,0x2a1f2da8,0xaa1f3000,0x2f64ada8,0x80003000,0xb000,0x30000000,0xb0008000,
0x2f640000,0xaa1f2da8,0xaa1fb000,0x2f642da8,0xada8b000,0x2da80000,0xada88000,0xada8b000,0xaa1f8000,0xaf64ada8,0x2f643000,0x2a1f2da8,
0x30003000,0x30003000,0x0,0x0,0xaf64b000,0xaa1fada8,0x2a1f8000,0x2f642da8,0x2da83000,0xada80000,0xada80000,0xada8b000,
0xaa1fb000,0x2f642da8,0x2f648000,0xaa1f2da8,0x3000,0x8000b000,0xb0000000,0x30008000,0x2a1fb000,0xaf642da8,0x2f648000,0xaa1fada8,
0xada83000,0x2da88000,0xada80000,0xada83000,0x2f64b000,0x2a1fada8,0x2a1f8000,0x2f64ada8,0xb0003000,0xb0003000,0x80000000,0x80000000,
0x2f64b000,0x2a1fada8,0xaa1f8000,0xaf642da8,0xada83000,0x2da80000,0x2da80000,0x2da8b000,0x2a1fb000,0xaf642da8,0xaf648000,0x2a1f2da8,
0x80003000,0xb000,0x30000000,0xb0008000,0xaa1fb000,0x2f642da8,0xaf648000,0x2a1fada8,0x2da83000,0xada88000,0x2da80000,0x2da83000,
0xaf64b000,0xaa1fada8,0xaa1f8000,0xaf64ada8,0x30003000,0x30003000,0x0,0x0,0x2a1f8000,0x2f642da8,0x2f643000,0x2a1f2da8,
0xada8b000,0x2da88000,0x2da88000,0x2da83000,0xaf640000,0x2a1fada8,0xaa1fb000,0x2f642da8,0x3000,0x8000b000,0xb0000000,0x30008000,
0x2f648000,0xaa1fada8,0xaa1f3000,0x2f64ada8,0x2da8b000,0xada80000,0x2da88000,0x2da8b000,0xaa1f0000,0xaf642da8,0x2f64b000,0x2a1fada8,
0xb0003000,0xb0003000,0x80000000,0x80000000,0xaa1f8000,0xaf642da8,0xaf643000,0xaa1f2da8,0x2da8b000,0xada88000,0xada88000,0xada83000,
0x2f640000,0xaa1fada8,0x2a1fb000,0xaf642da8,0x80003000,0xb000,0x30000000,0xb0008000,0xaf648000,0x2a1fada8,0x2a1f3000,0xaf64ada8,
0xada8b000,0x2da80000,0xada88000,0xada8b000,0x2a1f0000,0x2f642da8,0xaf64b000,0xaa1fada8,0x0,0x0,0xb000b000,0xb000b000,
0xaa1f0000,0xaf64ada8,0xaf64b000,0xaa1fada8,0xada80000,0xada8b000,0xada8b000,0x2da88000,0xaf640000,0x2a1fada8,0xaa1fb000,0x2f642da8,
0xb0000000,0x30008000,0x8000b000,0x3000,0xaf640000,0x2a1f2da8,0x2a1fb000,0xaf642da8,0xada80000,0xada83000,0x2da8b000,0xada80000,
0xaa1f0000,0xaf642da8,0x2f64b000,0x2a1fada8,0x80000000,0x80000000,0x3000b000,0x3000b000,0x2a1f0000,0x2f64ada8,0x2f64b000,0x2a1fada8,
0x2da80000,0x2da8b000,0x2da8b000,0xada88000,0x2f640000,0xaa1fada8,0x2a1fb000,0xaf642da8,0x30000000,0xb0008000,0xb000,0x80003000,
0x2f640000,0xaa1f2da8,0xaa1fb000,0x2f642da8,0x2da80000,0x2da83000,0xada8b000,0x2da80000,0x2a1f0000,0x2f642da8,0xaf64b000,0xaa1fada8,
0x0,0x0,0xb000b000,0xb000b000,0xaf64b000,0xaa1fada8,0x2a1f8000,0x2f642da8,0x2da88000,0x2da83000,0x2da83000,0xada80000,
0x2a1f3000,0xaf64ada8,0xaf640000,0x2a1fada8,0xb0000000,0x30008000,0x8000b000,0x3000,0x2a1fb000,0xaf642da8,0x2f648000,0xaa1fada8,
0x2da88000,0x2da8b000,0xada83000,0x2da88000,0xaf643000,0xaa1f2da8,0xaa1f0000,0xaf642da8,0x80000000,0x80000000,0x3000b000,0x3000b000,
0x2f64b000,0x2a1fada8,0xaa1f8000,0xaf642da8,0xada88000,0xada83000,0xada83000,0x2da80000,0xaa1f3000,0x2f64ada8,0x2f640000,0xaa1fada8,
0x30000000,0xb0008000,0xb000,0x80003000,0xaa1fb000,0x2f642da8,0xaf648000,0x2a1fada8,0xada88000,0xada8b000,0x2da83000,0xada88000,
0x2f643000,0x2a1f2da8,0x2a1f0000,0x2f642da8,0x0,0x0,0xb000b000,0xb000b000,0x2a1f8000,0x2f642da8,0x2f643000,0x2a1f2da8,
0xada80000,0xada8b000,0xada8b000,0x2da88000,0x2f648000,0xaa1f2da8,0x2a1f3000,0xaf64ada8,0xb0000000,0x30008000,0x8000b000,0x3000,
0x2f648000,0xaa1fada8,0xaa1f3000,0x2f64ada8,0xada80000,0xada83000,0x2da8b000,0xada80000,0x2a1f8000,0x2f64ada8,0xaf643000,0xaa1f2da8,
0x80000000,0x80000000,0x3000b000,0x3000b000,0xaa1f8000,0xaf642da8,0xaf643000,0xaa1f2da8,0x2da80000,0x2da8b000,0x2da8b000,0xada88000,
0xaf648000,0x2a1f2da8,0xaa1f3000,0x2f64ada8,0x30000000,0xb0008000,0xb000,0x80003000,0xaf648000,0x2a1fada8,0x2a1f3000,0xaf64ada8,
0x2da80000,0x2da83000,0xada8b000,0x2da80000,0xaa1f8000,0xaf64ada8,0x2f643000,0x2a1f2da8,0x0,0x0,0xb000b000,0xb000b000,
0x2f643000,0x2a1f2da8,0xaa1f0000,0xaf64ada8,0x2da88000,0x2da83000,0x2da83000,0xada80000,0xaa1fb000,0x2f642da8,0x2f648000,0xaa1f2da8,
0xb0000000,0x30008000,0x8000b000,0x3000,0xaa1f3000,0x2f64ada8,0xaf640000,0x2a1f2da8,0x2da88000,0x2da8b000,0xada83000,0x2da88000,
0x2f64b000,0x2a1fada8,0x2a1f8000,0x2f64ada8,0x80000000,0x80000000,0x3000b000,0x3000b000,0xaf643000,0xaa1f2da8,0x2a1f0000,0x2f64ada8,
0xada88000,0xada83000,0xada83000,0x2da80000,0x2a1fb000,0xaf642da8,0xaf648000,0x2a1f2da8,0x30000000,0xb0008000,0xb000,0x80003000,
0x2a1f3000,0xaf64ada8,0x2f640000,0xaa1f2da8,0xada88000,0xada8b000,0x2da83000,0xada88000,0xaf64b000,0xaa1fada8,0xaa1f8000,0xaf64ada8,
0xb000b000,0xb000b000,0x80008000,0x80008000,0xaf64b000,0xaa1fada8,0x2a1f8000,0x2f642da8,0xada8b000,0x2da88000,0x2da88000,0x2da83000,
0xaa1fb000,0x2f642da8,0x2f648000,0xaa1f2da8,0x8000b000,0x3000,0x30008000,0xb0000000,0x2a1fb000,0xaf642da8,0x2f648000,0xaa1fada8,
0x2da8b000,0xada80000,0x2da88000,0x2da8b000,0x2f64b000,0x2a1fada8,0x2a1f8000,0x2f64ada8,0x3000b000,0x3000b000,0x8000,0x8000,
0x2f64b000,0x2a1fada8,0xaa1f8000,0xaf642da8,0x2da8b000,0xada88000,0xada88000,0xada83000,0x2a1fb000,0xaf642da8,0xaf648000,0x2a1f2da8,
0xb000,0x80003000,0xb0008000,0x30000000,0xaa1fb000,0x2f642da8,0xaf648000,0x2a1fada8,0xada8b000,0x2da80000,0xada88000,0xada8b000,
0xaf64b000,0xaa1fada8,0xaa1f8000,0xaf64ada8,0xb000b000,0xb000b000,0x80008000,0x80008000,0x2a1f8000,0x2f642da8,0x2f643000,0x2a1f2da8,
0x2da83000,0xada80000,0xada80000,0xada8b000,0xaf640000,0x2a1fada8,0xaa1fb000,0x2f642da8,0x8000b000,0x3000,0x30008000,0xb0000000,
0x2f648000,0xaa1fada8,0xaa1f3000,0x2f64ada8,0xada83000,0x2da88000,0xada80000,0xada83000,0xaa1f0000,0xaf642da8,0x2f64b000,0x2a1fada8,
0x3000b000,0x3000b000,0x8000,0x8000,0xaa1f8000,0xaf642da8,0xaf643000,0xaa1f2da8,0xada83000,0x2da80000,0x2da80000,0x2da8b000,
0x2f640000,0xaa1fada8,0x2a1fb000,0xaf642da8,0xb000,0x80003000,0xb0008000,0x30000000,0xaf648000,0x2a1fada8,0x2a1f3000,0xaf64ada8,
0x2da83000,0xada88000,0x2da80000,0x2da83000,0x2a1f0000,0x2f642da8,0xaf64b000,0xaa1fada8,0xb000b000,0xb000b000,0x80008000,0x80008000,
0x2f643000,0x2a1f2da8,0xaa1f0000,0xaf64ada8,0xada8b000,0x2da88000,0x2da88000,0x2da83000,0x2a1f3000,0xaf64ada8,0xaf640000,0x2a1fada8,
0x8000b000,0x3000,0x30008000,0xb0000000,0xaa1f3000,0x2f64ada8,0xaf640000,0x2a1f2da8,0x2da8b000,0xada80000,0x2da88000,0x2da8b000,
0xaf643000,0xaa1f2da8,0xaa1f0000,0xaf642da8,0x3000b000,0x3000b000,0x8000,0x8000,0xaf643000,0xaa1f2da8,0x2a1f0000,0x2f64ada8,
0x2da8b000,0xada88000,0xada88000,0xada83000,0xaa1f3000,0x2f64ada8,0x2f640000,0xaa1fada8,0xb000,0x80003000,0xb0008000,0x30000000,
0x2a1f3000,0xaf64ada8,0x2f640000,0xaa1f2da8,0xada8b000,0x2da80000,0xada88000,0xada8b000,0x2f643000,0x2a1f2da8,0x2a1f0000,0x2f642da8,
0xb000b000,0xb000b000,0x80008000,0x80008000,0xaa1f0000,0xaf64ada8,0xaf64b000,0xaa1fada8,0x2da83000,0xada80000,0xada80000,0xada8b000,
0x2f648000,0xaa1f2da8,0x2a1f3000,0xaf64ada8,0x8000b000,0x3000,0x30008000,0xb0000000,0xaf640000,0x2a1f2da8,0x2a1fb000,0xaf642da8,
0xada83000,0x2da88000,0xada80000,0xada83000,0x2a1f8000,0x2f64ada8,0xaf643000,0xaa1f2da8,0x3000b000,0x3000b000,0x8000,0x8000,
0x2a1f0000,0x2f64ada8,0x2f64b000,0x2a1fada8,0xada83000,0x2da80000,0x2da80000,0x2da8b000,0xaf648000,0x2a1f2da8,0xaa1f3000,0x2f64ada8,
0xb000,0x80003000,0xb0008000,0x30000000,0x2f640000,0xaa1f2da8,0xaa1fb000,0x2f642da8,0x2da83000,0xada88000,0x2da80000,0x2da83000,
0xaa1f8000,0xaf64ada8,0x2f643000,0x2a1f2da8,0x80008000,0x80008000,0x30003000,0x30003000,0x2a1f8000,0x2f642da8,0x2f643000,0x2a1f2da8,
0x2da88000,0x2da83000,0x2da83000,0xada80000,0x2f648000,0xaa1f2da8,0x2a1f3000,0xaf64ada8,0x30008000,0xb0000000,0x3000,0x8000b000,
0x2f648000,0xaa1fada8,0xaa1f3000,0x2f64ada8,0x2da88000,0x2da8b000,0xada83000,0x2da88000,0x2a1f8000,0x2f64ada8,0xaf643000,0xaa1f2da8,
0x8000,0x8000,0xb0003000,0xb0003000,0xaa1f8000,0xaf642da8,0xaf643000,0xaa1f2da8,0xada88000,0xada83000,0xada83000,0x2da80000,
0xaf648000,0x2a1f2da8,0xaa1f3000,0x2f64ada8,0xb0008000,0x30000000,0x80003000,0xb000,0xaf648000,0x2a1fada8,0x2a1f3000,0xaf64ada8,
0xada88000,0xada8b000,0x2da83000,0xada88000,0xaa1f8000,0xaf64ada8,0x2f643000,0x2a1f2da8,0x80008000,0x80008000,0x30003000,0x30003000,
0x2f643000,0x2a1f2da8,0xaa1f0000,0xaf64ada8,0xada80000,0xada8b000,0xada8b000,0x2da88000,0xaa1fb000,0x2f642da8,0x2f648000,0xaa1f2da8,
0x30008000,0xb0000000,0x3000,0x8000b000,0xaa1f3000,0x2f64ada8,0xaf640000,0x2a1f2da8,0xada80000,0xada83000,0x2da8b000,0xada80000,
0x2f64b000,0x2a1fada8,0x2a1f8000,0x2f64ada8,0x8000,0x8000,0xb0003000,0xb0003000,0xaf643000,0xaa1f2da8,0x2a1f0000,0x2f64ada8,
0x2da80000,0x2da8b000,0x2da8b000,0xada88000,0x2a1fb000,0xaf642da8,0xaf648000,0x2a1f2da8,0xb0008000,0x30000000,0x80003000,0xb000,
0x2a1f3000,0xaf64ada8,0x2f640000,0xaa1f2da8,0x2da80000,0x2da83000,0xada8b000,0x2da80000,0xaf64b000,0xaa1fada8,0xaa1f8000,0xaf64ada8,
0x80008000,0x80008000,0x30003000,0x30003000,0xaa1f0000,0xaf64ada8,0xaf64b000,0xaa1fada8,0x2da88000,0x2da83000,0x2da83000,0xada80000,
0xaf640000,0x2a1fada8,0xaa1fb000,0x2f642da8,0x30008000,0xb0000000,0x3000,0x8000b000,0xaf640000,0x2a1f2da8,0x2a1fb000,0xaf642da8,
0x2da88000,0x2da8b000,0xada83000,0x2da88000,0xaa1f0000,0xaf642da8,0x2f64b000,0x2a1fada8,0x8000,0x8000,0xb0003000,0xb0003000,
0x2a1f0000,0x2f64ada8,0x2f64b000,0x2a1fada8,0xada88000,0xada83000,0xada83000,0x2da80000,0x2f640000,0xaa1fada8,0x2a1fb000,0xaf642da8,
0xb0008000,0x30000000,0x80003000,0xb000,0x2f640000,0xaa1f2da8,0xaa1fb000,0x2f642da8,0xada88000,0xada8b000,0x2da83000,0xada88000,
0x2a1f0000,0x2f642da8,0xaf64b000,0xaa1fada8,0x80008000,0x80008000,0x30003000,0x30003000,0xaf64b000,0xaa1fada8,0x2a1f8000,0x2f642da8,
0xada80000,0xada8b000,0xada8b000,0x2da88000,0x2a1f3000,0xaf64ada8,0xaf640000,0x2a1fada8,0x30008000,0xb0000000,0x3000,0x8000b000,
0x2a1fb000,0xaf642da8,0x2f648000,0xaa1fada8,0xada80000,0xada83000,0x2da8b000,0xada80000,0xaf643000,0xaa1f2da8,0xaa1f0000,0xaf642da8,
0x8000,0x8000,0xb0003000,0xb0003000,0x2f64b000,0x2a1fada8,0xaa1f8000,0xaf642da8,0x2da80000,0x2da8b000,0x2da8b000,0xada88000,
0xaa1f3000,0x2f64ada8,0x2f640000,0xaa1fada8,0xb0008000,0x30000000,0x80003000,0xb000,0xaa1fb000,0x2f642da8,0xaf648000,0x2a1fada8,
0x2da80000,0x2da83000,0xada8b000,0x2da80000,0x2f643000,0x2a1f2da8,0x2a1f0000,0x2f642da8,
};


typedef const __attribute__((address_space(1))) u4* gu4p;
DI u4 gload16(const void* p) { return *(gu4p)(uintptr_t)p; }
DI float bflo(u32 v) { return __uint_as_float(v << 16); }
DI float bfhi(u32 v) { return __uint_as_float(v & 0xffff0000u); }
DI float bf2f(u32 v) { return __uint_as_float(v << 16); }
DI u32 pkbf(float a, float b) { f2 t = {a, b}; return __builtin_bit_cast(u32, __builtin_convertvector(t, b2)); }
DI u32 pkbf(f2 t) { return __builtin_bit_cast(u32, __builtin_convertvector(t, b2)); }
DI float rbf(float a) { return bflo(pkbf(a, a)); }
DI f2 unbf(u32 v) { return f2{bflo(v), bfhi(v)}; }
DI f2 rbf2(f2 t) { return unbf(pkbf(t)); }
DI u32 pkh(float a, float b) { f2 t = {a, b}; return __builtin_bit_cast(u32, __builtin_convertvector(t, h2)); }
DI u32 pkh(f2 t) { return __builtin_bit_cast(u32, __builtin_convertvector(t, h2)); }
DI float vcos(float rev) { return __builtin_amdgcn_cosf(rev); }
DI float vsin(float rev) { return __builtin_amdgcn_sinf(rev); }
DI f4 mfma_h(u4 a, u4 b, f4 c) {
  return __builtin_amdgcn_mfma_f32_16x16x32_f16(__builtin_bit_cast(h8, a), __builtin_bit_cast(h8, b), c, 0, 0, 0);
}
DI f4 mfma_b(u4 a, u4 b, f4 c) {
  return __builtin_amdgcn_mfma_f32_16x16x32_bf16(__builtin_bit_cast(b8, a), __builtin_bit_cast(b8, b), c, 0, 0, 0);
}
DI u2 trread(const char* p) {
  s4 v = __builtin_amdgcn_ds_read_tr16_b64_v4i16((lds_s4p)p);
  return __builtin_bit_cast(u2, v);
}
DI u4 cat(u2 a, u2 b) { return u4{a[0], a[1], b[0], b[1]}; }
DI u4 neg_h8(u4 a) { return a ^ 0x80008000u; }
DI int tok_row(int tile, int c) { return 4 * tile + 2 * (c >> 3) + 512 * ((c & 7) >> 1) + (c & 1); }
DI u4 bload(__amdgpu_buffer_rsrc_t r, int voff, int soff) {
  return __builtin_bit_cast(u4, __builtin_amdgcn_raw_buffer_load_b128(r, voff, soff, 0));
}
DI f2 lo2(f4 v) { return f2{v[0], v[1]}; }
DI f2 hi2(f4 v) { return f2{v[2], v[3]}; }
DI u4 cmul_pack_h(f4 re, f4 im, f4 c, f4 s) {
  const f2 r0 = lo2(re) * lo2(c) - lo2(im) * lo2(s), r1 = hi2(re) * hi2(c) - hi2(im) * hi2(s);
  const f2 i0 = lo2(re) * lo2(s) + lo2(im) * lo2(c), i1 = hi2(re) * hi2(s) + hi2(im) * hi2(c);
  return u4{pkh(r0), pkh(r1), pkh(i0), pkh(i1)};
}

DI f2 cmul(f2 a, f2 b) {
  return f2{a[0], a[0]} * b + f2{a[1], a[1]} * f2{-b[1], b[0]};
}

template <int CTRL> DI float dppf(float v) {
  return __builtin_bit_cast(float, __builtin_amdgcn_update_dpp(0, __builtin_bit_cast(int, v), CTRL, 0xf, 0xf, false));
}
DI float swap16_add(float v) {
  u32 u = __float_as_uint(v);
  auto r = __builtin_amdgcn_permlane16_swap(u, u, false, false);
  return __uint_as_float(r[0]) + __uint_as_float(r[1]);
}
DI float swap32_add(float v) {
  u32 u = __float_as_uint(v);
  auto r = __builtin_amdgcn_permlane32_swap(u, u, false, false);
  return __uint_as_float(r[0]) + __uint_as_float(r[1]);
}
DI float swap16_max(float v) {
  u32 u = __float_as_uint(v);
  auto r = __builtin_amdgcn_permlane16_swap(u, u, false, false);
  return fmaxf(__uint_as_float(r[0]), __uint_as_float(r[1]));
}
DI float swap32_max(float v) {
  u32 u = __float_as_uint(v);
  auto r = __builtin_amdgcn_permlane32_swap(u, u, false, false);
  return fmaxf(__uint_as_float(r[0]), __uint_as_float(r[1]));
}
DI float wave_sum(float v) {
  v += dppf<0x128>(v); v += dppf<0x124>(v); v += dppf<0x122>(v); v += dppf<0x121>(v);
  return swap32_add(swap16_add(v));
}
DI void wave_sum2(float& a, float& b) {
  a += dppf<0x128>(a); b += dppf<0x128>(b);
  a += dppf<0x124>(a); b += dppf<0x124>(b);
  a += dppf<0x122>(a); b += dppf<0x122>(b);
  a += dppf<0x121>(a); b += dppf<0x121>(b);
  a = swap16_add(a); b = swap16_add(b);
  a = swap32_add(a); b = swap32_add(b);
}
DI float wave_max(float v) {
  v = fmaxf(v, dppf<0x128>(v)); v = fmaxf(v, dppf<0x124>(v)); v = fmaxf(v, dppf<0x122>(v)); v = fmaxf(v, dppf<0x121>(v));
  return swap32_max(swap16_max(v));
}
DI constexpr int offt(int mt) { return mt == 0 ? 0 : (mt == 1 ? 16 : 24); }
DI int xcd_map(int b, int per) { return (b & 7) * per + (b >> 3); }

constexpr int K0_GEMV = 240;
constexpr int K0_WD = 216;

DI void job_gemv(int bid, const u16* __restrict__ c, const u16* __restrict__ wmod, float* __restrict__ mod, char* lds) {
  const int cb = bid < 80 ? ((bid & 7) * 10 + (bid >> 3)) : 80 + ((bid - 80) & 7) * 20 + ((bid - 80) >> 3);
  const int n0 = cb * 16;
  const int t = threadIdx.x, half = t & 1, rl = t >> 1;
  u4 w[5];
  u32 cv[5];
#pragma unroll
  for (int i = 0; i < 5; i++) w[i] = *(const u4*)(wmod + (size_t)(rl + 128 * i) * 3840 + n0 + 8 * half);
#pragma unroll
  for (int i = 0; i < 5; i++) cv[i] = c[rl + 128 * i];
  f2 acc[4];
#pragma unroll
  for (int e = 0; e < 4; e++) acc[e] = f2{0.f, 0.f};
#pragma unroll
  for (int i = 0; i < 5; i++) {
    const float x = bf2f(cv[i]);
    const float s = rbf(x * __builtin_amdgcn_rcpf(1.0f + __expf(-x)));
#pragma unroll
    for (int e = 0; e < 4; e++) acc[e] += unbf(w[i][e]) * s;
  }
  float a[8] = {acc[0][0], acc[0][1], acc[1][0], acc[1][1], acc[2][0], acc[2][1], acc[3][0], acc[3][1]};
#pragma unroll
  for (int e = 0; e < 8; e++) {
    a[e] += dppf<0x4e>(a[e]);
    a[e] += dppf<0x124>(a[e]);
    a[e] += dppf<0x128>(a[e]);
    a[e] = swap32_add(swap16_add(a[e]));
  }
  float* red = (float*)lds;
  const int wv = t >> 6, ln = t & 63;
  if (ln < 2) {
    *(f4*)(red + wv * 16 + ln * 8) = f4{a[0], a[1], a[2], a[3]};
    *(f4*)(red + wv * 16 + ln * 8 + 4) = f4{a[4], a[5], a[6], a[7]};
  }
  __syncthreads();
  if (t < 16) {
    float v = rbf((red[t] + red[16 + t]) + (red[32 + t] + red[48 + t]));
    const int n = n0 + t, which = n / 640;
    if (which == 1 || which == 4) v = rbf(1.0f + v);
    mod[n] = v;
    if (which < 2) {
      const int d = n - 640 * which;
      ((u16*)((char*)mod + (WS_MODP - WS_MOD)))[(d >> 1) * 4 + (which == 1 ? 0 : 2) + (d & 1)] = (u16)(__float_as_uint(v) >> 16);
    }
  }
}

constexpr int WT_RS = 104, WT_LDS = 128 * WT_RS;
constexpr int WTW_LDS = 32 * WT_RS;
DI void wtile_load(int id, int wq, int l, const u16* __restrict__ wr, const u16* __restrict__ wi, u4 (&v)[3], bool on = true) {
  const int x = id & 7, m = (id >> 3) & 7, ch = id >> 6;
  int off[3] = {0, 0, 0};
  if (on) {
#pragma unroll
    for (int it = 0; it < 3; it++) {
      const int idx = it * 64 + l;
      const int seg = idx / 6, pc = idx - 6 * seg;
      const int z = (seg >> 3) & 1, e = (seg >> 2) & 1, g = seg & 3;
      const int R = 128 * m + 64 * e + 16 * ch + wq + 4 * g + 1024 * z;
      off[it] = R * DF_ + (pc == 5 ? 313 : 40 * x + 8 * pc);
    }
  }
  v[0] = *(const u4a2*)(wr + off[0]);
  v[1] = *(const u4a2*)((l < 32 ? wr : wi) + off[1]);
  v[2] = *(const u4a2*)(wi + off[2]);
}
DI void wtile_finish(int id, int wq, int l, const u4 (&v)[3], char* __restrict__ wt, char* lds) {
  const int x = id & 7, m = (id >> 3) & 7, ch = id >> 6;
#pragma unroll
  for (int it = 0; it < 3; it++) {
    const int idx = it * 64 + l;
    const int seg = idx / 6, pc = idx - 6 * seg;
    char* p = lds + seg * WT_RS + pc * 16;
    *(u2*)p = u2{v[it][0], v[it][1]};
    *(u2*)(p + 8) = u2{v[it][2], v[it][3]};
  }
  const int i16 = l & 15, g4 = l >> 4;
  const int rsel = i16 >> 2;
  const int rowoff = ((rsel & 1) * 16 + (rsel >> 1) * 8 + g4) * WT_RS + 8 * (i16 & 3);
#pragma unroll
  for (int colb = 0; colb < 3; colb++) {
    const int k1 = wq, c = 4 * ch + g4;
    const char* p = lds + rowoff + colb * 32;
    const u2 a = trread(p), b = trread(p + 4 * WT_RS);
    const int kl = 16 * colb + i16, kd = kl < 40 ? 40 * x + kl : 320;
    const u4 o = u4{__builtin_amdgcn_perm(b[0], a[0], 0x05040100u), __builtin_amdgcn_perm(b[0], a[0], 0x07060302u),
                    __builtin_amdgcn_perm(b[1], a[1], 0x05040100u), __builtin_amdgcn_perm(b[1], a[1], 0x07060302u)};
    if (kl < 40 || (x == 7 && kl == 47)) *(u4*)(wt + ((size_t)(kd * 4 + k1) * 2 + (m & 1)) * 1024 + (c + 16 * (m >> 1)) * 16) = o;
  }
}
constexpr int WD_RS = 96 + 8, WDW_LDS = 32 * WD_RS, WD_LDS = 4 * WDW_LDS;
DI void wd_load(int id, int wq, int l, const u16* __restrict__ wd, u4 (&v)[3], bool on = true) {
  const int s = id >> 2, qd = id & 3;
  int off[3] = {0, 0, 0};
  if (on) {
#pragma unroll
    for (int z = 0; z < 3; z++) {
      const int fr = wq + 4 * z < 10 ? wq + 4 * z : wq;
      off[z] = (32 * s + (l >> 1)) * D_ + 160 * qd + 16 * fr + 8 * (l & 1);
    }
  }
#pragma unroll
  for (int z = 0; z < 3; z++) v[z] = *(const u4*)(wd + off[z]);
}
DI void wd_finish(int id, int wq, int l, const u4 (&v)[3], char* __restrict__ dst, char* lds) {
  const int s = id >> 2, qd = id & 3;
#pragma unroll
  for (int z = 0; z < 3; z++) {
    char* p = lds + (l >> 1) * WD_RS + z * 32 + (l & 1) * 16;
    *(u2*)p = u2{v[z][0], v[z][1]};
    *(u2*)(p + 8) = u2{v[z][2], v[z][3]};
  }
  const int i16 = l & 15, g4 = l >> 4;
#pragma unroll
  for (int z = 0; z < 3; z++) {
    const int fr = wq + 4 * z;
    if (fr < 10) {
      const int cl = fr / 5, i = fr % 5;
      const char* p = lds + (8 * g4 + (i16 >> 2)) * WD_RS + z * 32 + 8 * (i16 & 3);
      const u2 a = trread(p), b = trread(p + 4 * WD_RS);
      *(u4*)(dst + ((size_t)(((2 * qd + cl) * 54 + s) * 5 + i)) * 1024 + l * 16) = cat(a, b);
    }
  }
}
__global__ __launch_bounds__(256) void k0_mod(const u16* __restrict__ c, const u16* __restrict__ wmod, char* __restrict__ ws) {
  __shared__ __attribute__((aligned(16))) char lds[256];
  if (blockIdx.x == 0 && threadIdx.x < 64) *(u32*)(ws + WS_CNT + 256 * threadIdx.x) = 0u;
  job_gemv(blockIdx.x, c, wmod, (float*)(ws + WS_MOD), lds);
}

DI constexpr int himg(int rho) { return 96 * rho + 128 * (rho >> 3); }
constexpr int K1_H = 0, K1_CA = 14336, K1_RQ = K1_CA + 15360, K1_O = K1_RQ + 4096, K1_SW = K1_O + ROWP * 4,
              K1_LDS = K1_SW + WT_LDS;
template <int K1V>
DI void k1_stage1(const u4 (&A)[4][3], const u4 (&R)[4], f4 (&dre)[3], f4 (&dim)[3]) {
#pragma unroll
  for (int kap = 0; kap < 4; kap++)
#pragma unroll
    for (int mt = 0; mt < 3; mt++) {
      dre[mt] = mfma_h(A[kap][mt], R[(kap * K1V) & 3], dre[mt]);
      dim[mt] = mfma_h(A[kap][mt], R[(kap * K1V + 1) & 3], dim[mt]);
    }
}
__global__ __launch_bounds__(512, 2) void k1_fwd(const u16* __restrict__ x, const u16* __restrict__ wr,
                                                 const u16* __restrict__ wi, char* __restrict__ ws) {
  __shared__ __attribute__((aligned(16))) char lds[K1_LDS];
  char* H = lds + K1_H;
  char* CA = lds + K1_CA;
  u32* O = (u32*)(lds + K1_O);
  const int s2 = xcd_map(blockIdx.x, 32);
  const int t = threadIdx.x, l = t & 63, l15 = l & 15, g4 = l >> 4;
  const int wv = __builtin_amdgcn_readfirstlane(t >> 6);
  const int row = 2 * s2 + 512 * (wv >> 1) + (wv & 1);
  u32 xv[5];
  u2 mv[5];
#pragma unroll
  for (int i = 0; i < 5; i++) xv[i] = *(const u32*)(x + (size_t)row * D_ + 128 * i + 2 * l);
#pragma unroll
  for (int i = 0; i < 5; i++) mv[i] = *(const u2*)(ws + WS_MODP + (64 * i + l) * 8);
  SB();
  u4 tb[3];
  {
    const char* s0 = (const char*)CA2_TAB + wv * 1024;
    const char* s1 = wv < 7 ? (const char*)CA2_TAB + (8 + wv) * 1024 : (const char*)RQ_TAB;
    const char* s2p = (const char*)RQ_TAB + (wv < 3 ? wv + 1 : 0) * 1024;
    tb[0] = gload16(s0 + l * 16);
    tb[1] = gload16(s1 + l * 16);
    tb[2] = gload16(s2p + l * 16);
    SB();
  }
  u4 sw[3];
  wtile_load(blockIdx.x, wv & 3, l, wr, wi, sw, wv >= 4);
  SB();
  int haddr[5];
#pragma unroll
  for (int i = 0; i < 5; i++) {
    const int d = 128 * i + 2 * l, n1 = (d * 1639) >> 16, n2 = d - 40 * n1;
    haddr[i] = himg(16) * wv + 96 * n1 + 128 * (n1 >> 3) + 2 * n2;
    PIN(haddr[i]);
  }
  const int k1 = wv & 3;
  f4 twc[3], tws[3];
  int p0, p1, pm0, pm1, taddr;
  const int alt = 2 * ((4 - k1) & 3) + 1;
  if (wv < 4) {
    const float fl = (float)l15 * (1.0f / 640.0f);
    const f2 st = f2{vcos(fl), -vsin(fl)};
    const float a0 = fl * (float)(4 * g4), a1 = fl * (float)(16 + 4 * g4), a2 = fl * (float)(24 + 4 * g4);
    f2 w[3] = {f2{vcos(a0), -vsin(a0)}, f2{vcos(a1), -vsin(a1)}, f2{vcos(a2), -vsin(a2)}};
#pragma unroll
    for (int mt = 0; mt < 3; mt++) {
      const f2 w1 = cmul(w[mt], st), w2 = cmul(w1, st), w3 = cmul(w2, st);
      twc[mt] = f4{w[mt][0], w1[0], w2[0], w3[0]};
      tws[mt] = f4{w[mt][1], w1[1], w2[1], w3[1]};
    }
    const int lb = 8 * (l15 + 32 * g4);
    p0 = lb + 2 * k1;
    p1 = 5120 + alt - lb - 128 - 4096;
    pm0 = (lb <= 8 * 64) ? p0 + 2048 : p1 + 128 + 2048;
    pm1 = (lb + 128 <= 8 * 64) ? p0 + 2048 + 128 : p1 + 2048;
    taddr = himg(8 * g4 + (l15 >> 2)) + 8 * (l15 & 3);
#pragma unroll
    for (int mt = 0; mt < 3; mt++) { PIN(twc[mt]); PIN(tws[mt]); }
    PIN(p0); PIN(p1); PIN(pm0); PIN(pm1); PIN(taddr);
  }
  SB();
#pragma unroll
  for (int i = 0; i < 5; i++) PIN(xv[i]);
  f2 xf[5];
  const float x0 = __builtin_bit_cast(float, __builtin_amdgcn_readfirstlane(xv[0] << 16));
  f2 sum2 = {0.f, 0.f}, sq2 = {0.f, 0.f};
#pragma unroll
  for (int i = 0; i < 5; i++) {
    xf[i] = unbf(xv[i]) - x0;
    sum2 += xf[i];
    sq2 += xf[i] * xf[i];
  }
  float sum = sum2[0] + sum2[1], sq = sq2[0] + sq2[1];
  u32 mb = 0;
#pragma unroll
  for (int i = 0; i < 5; i++) {
    const u2 ab = mv[i] & 0x7fff7fffu;
    mb = max(mb, max(max(ab[0] << 16, ab[0] & 0x7fff0000u), max(ab[1] << 16, ab[1] & 0x7fff0000u)));
  }
  float mxf = __uint_as_float(mb);
  sum += dppf<0x128>(sum); sq += dppf<0x128>(sq); mxf = fmaxf(mxf, dppf<0x128>(mxf));
  sum += dppf<0x124>(sum); sq += dppf<0x124>(sq); mxf = fmaxf(mxf, dppf<0x124>(mxf));
  sum += dppf<0x122>(sum); sq += dppf<0x122>(sq); mxf = fmaxf(mxf, dppf<0x122>(mxf));
  sum += dppf<0x121>(sum); sq += dppf<0x121>(sq); mxf = fmaxf(mxf, dppf<0x121>(mxf));
  sum = swap16_add(sum); sq = swap16_add(sq); mxf = swap16_max(mxf);
  sum = swap32_add(sum); sq = swap32_add(sq); mxf = swap32_max(mxf);
  const float mean = sum * (1.0f / 640.0f);
  const float var = fmaxf(sq * (1.0f / 640.0f) - mean * mean, 0.f);
  const float rstd = __builtin_amdgcn_rsqf(var + 1e-5f);
  int ex = (int)((__float_as_uint(mxf) >> 23) & 255) - 127 + 2;
  ex = ex < -100 ? -100 : (ex > 100 ? 100 : ex);
  const float sh = __uint_as_float((u32)(127 - ex) << 23);
  if (blockIdx.x == 0 && t == 0) *(float*)(ws + WS_SCL) = __uint_as_float((u32)(127 + ex) << 23);
#pragma unroll
  for (int i = 0; i < 5; i++) {
    const f2 k = unbf(mv[i][0]) * (rstd * sh);
    *(u32*)(H + haddr[i]) = pkh((xf[i] - mean) * k + unbf(mv[i][1]) * sh);
  }
  *(u4*)(CA + wv * 1024 + l * 16) = tb[0];
  *(u4*)(CA + (wv + 8) * 1024 + l * 16) = tb[1];
  if (wv < 3) *(u4*)(CA + (wv + 16) * 1024 + l * 16) = tb[2];
  __syncthreads();
  if (wv < 4) {
    u4 R[4];
    const char* rq = lds + K1_RQ + l * 16;
#pragma unroll
    for (int z = 0; z < 4; z++) R[z] = *(const u4*)(rq + z * 1024);
    u4 A[4][3];
#pragma unroll
    for (int kap = 0; kap < 4; kap++)
#pragma unroll
      for (int mt = 0; mt < 3; mt++) {
        const char* q0 = H + taddr + himg(32 * kap) + 2 * offt(mt);
        A[kap][mt] = cat(trread(q0), trread(q0 + 384));
      }
    u4 ca[15];
#pragma unroll
    for (int z = 0; z < 15; z++) ca[z] = *(const u4*)(CA + z * 1024 + l * 16);
    SB();
    f4 dre[3], dim[3];
#pragma unroll
    for (int mt = 0; mt < 3; mt++) { dre[mt] = f4{0, 0, 0, 0}; dim[mt] = f4{0, 0, 0, 0}; }
    if (k1 == 0) k1_stage1<0>(A, R, dre, dim);
    else if (k1 == 1) k1_stage1<1>(A, R, dre, dim);
    else if (k1 == 2) k1_stage1<2>(A, R, dre, dim);
    else k1_stage1<3>(A, R, dre, dim);
    u4 Bf[3];
#pragma unroll
    for (int mt = 0; mt < 3; mt++) Bf[mt] = cmul_pack_h(dre[mt], dim[mt], twc[mt], tws[mt]);
    f4 acc[5];
#pragma unroll
    for (int T = 0; T < 5; T++) acc[T] = f4{0, 0, 0, 0};
#pragma unroll
    for (int mt = 0; mt < 3; mt++)
#pragma unroll
      for (int T = 0; T < 5; T++) acc[T] = mfma_h(ca[T * 3 + mt], Bf[mt], acc[T]);
    O[p0] = pkh(acc[0][0], acc[0][1]);
    O[p0 + 128] = pkh(acc[0][2], acc[0][3]);
    O[p0 + 1024] = pkh(acc[1][0], acc[1][1]);
    O[p0 + 1024 + 128] = pkh(acc[1][2], acc[1][3]);
    const u32 v20 = pkh(acc[2][0], acc[2][1]);
    O[pm0] = v20;
    O[pm1] = pkh(acc[2][2], acc[2][3]);
    O[p1 + 128 + 1024] = pkh(acc[3][0], acc[3][1]);
    O[p1 + 1024] = pkh(acc[3][2], acc[3][3]);
    O[p1 + 128] = pkh(acc[4][0], acc[4][1]);
    O[p1] = pkh(acc[4][2], acc[4][3]);
    if ((l & 31) == 0) O[l == 0 ? alt : 8 * 320 + alt] = l == 0 ? pkh(acc[0][0], acc[0][1]) : v20;
  } else {
    wtile_finish(blockIdx.x, wv & 3, l, sw, ws + WS_WT, lds + K1_SW + (wv & 3) * WTW_LDS);
  }
  __syncthreads();
  char* T1 = ws + WS_T1 + (size_t)s2 * 32 + (size_t)(t >> 1) * 8192 + (t & 1) * 16;
  *(u4*)(T1) = *(const u4*)(O + 4 * t);
  if (t < 642 - 512) *(u4*)(T1 + (size_t)256 * 8192) = *(const u4*)(O + 4 * t + 2048);
}

constexpr int K2_GRID = 321 + K0_WD;
__global__ __launch_bounds__(256, 2) void k2_filter(const u16* __restrict__ wd, char* __restrict__ ws) {
  __shared__ __attribute__((aligned(16))) u32 O[WD_LDS / 4 > 256 * 8 ? WD_LDS / 4 : 256 * 8];
  const int b = blockIdx.x;
  if (b >= 321) {
    u4 v[3];
    const int wq = threadIdx.x >> 6, ln = threadIdx.x & 63;
    wd_load(b - 321, wq, ln, wd, v);
    wd_finish(b - 321, wq, ln, v, ws + WS_WD, (char*)O + wq * WDW_LDS);
    return;
  }
  const int g = b < 320 ? (b & 7) * 40 + (b >> 3) : b;
  const int t = threadIdx.x, l = t & 63, l15 = l & 15, g4 = l >> 4;
  const int k1 = __builtin_amdgcn_readfirstlane(t >> 6);
  const char* T1 = ws + WS_T1 + (size_t)g * 8192 + 8 * k1;
  u2 pv[4];
#pragma unroll
  for (int i = 0; i < 4; i++) pv[i] = *(const u2*)(T1 + (64 * g4 + l15) * 32 + i * 512);
  const char* Wt = ws + WS_WT + (size_t)(g * 4 + k1) * 2048;
  u4 w0 = *(const u4*)(Wt + l * 16), w1 = *(const u4*)(Wt + 1024 + l * 16);
  SB();
  const float u4c = (float)(4 * l15 + k1);
  const float fg = (float)(4 * g4);
  f2 t1[4], t3[4], ph[4];
  {
    const float a1s = u4c * (1.0f / 1024.0f), a1b = fg * a1s;
    const f2 s1 = f2{vcos(a1s), -vsin(a1s)};
    t1[0] = f2{vcos(a1b), -vsin(a1b)};
    const float fl = (float)l15;
    const float a3s = fl * (4.0f / 1024.0f), a3b = fl * (4.0f * fg + (float)k1) * (1.0f / 1024.0f);
    const f2 s3 = f2{vcos(a3s), vsin(a3s)};
    t3[0] = f2{vcos(a3b), vsin(a3b)};
    const float apb = (u4c + 64.0f * fg) * (1.0f / 2048.0f);
    const f2 sp = f2{0.98078528040323043f, 0.19509032201612825f};
    ph[0] = f2{vcos(apb), vsin(apb)};
#pragma unroll
    for (int r = 1; r < 4; r++) { t1[r] = cmul(t1[r - 1], s1); t3[r] = cmul(t3[r - 1], s3); ph[r] = cmul(ph[r - 1], sp); }
  }
  f4 t1c = f4{t1[0][0], t1[1][0], t1[2][0], t1[3][0]}, t1s = f4{t1[0][1], t1[1][1], t1[2][1], t1[3][1]};
  f4 t3c = f4{t3[0][0], t3[1][0], t3[2][0], t3[3][0]}, t3s = f4{t3[0][1], t3[1][1], t3[2][1], t3[3][1]};
  f4 pc = f4{ph[0][0], ph[1][0], ph[2][0], ph[3][0]}, ps = f4{ph[0][1], ph[1][1], ph[2][1], ph[3][1]};
  int oaddr = (64 * g4 + l15) * 8 + 2 * k1;
  PIN(t1c); PIN(t1s); PIN(t3c); PIN(t3s); PIN(pc); PIN(ps); PIN(oaddr);
  u4 B1re, B1im, A2re, A2im, B3re, B3im, A4re, A4im;
  {
    const float st1 = u4c * (1.0f / 64.0f), fl = (float)l15;
    const float st2 = fl * (1.0f / 16.0f), b4 = fl * (4.0f * fg + (float)k1) * (1.0f / 64.0f);
    f2 c1[2], s1[2], c2[2], s2[2], c4[2], s4[2];
#pragma unroll
    for (int i = 0; i < 4; i++) {
      const float a1 = (fg + (float)i) * st1, a2 = (fg + (float)i) * st2, a4 = fmaf((float)i, st2, b4);
      c1[i >> 1][i & 1] = vcos(a1); s1[i >> 1][i & 1] = vsin(a1);
      c2[i >> 1][i & 1] = vcos(a2); s2[i >> 1][i & 1] = vsin(a2);
      c4[i >> 1][i & 1] = vcos(a4); s4[i >> 1][i & 1] = vsin(a4);
    }
#pragma unroll
    for (int p = 0; p < 2; p++) {
      c1[p] *= 0.25f; s1[p] *= 0.25f; c2[p] *= 0.125f; s2[p] *= 0.125f; c4[p] *= 0.25f; s4[p] *= 0.25f;
      B1re[2 * p] = pkh(c1[p][0], s1[p][0]); B1re[2 * p + 1] = pkh(c1[p][1], s1[p][1]);
      B1im[2 * p] = pkh(-s1[p][0], c1[p][0]); B1im[2 * p + 1] = pkh(-s1[p][1], c1[p][1]);
    }
    const u32 C0 = pkh(c2[0]), C1 = pkh(c2[1]), S0 = pkh(s2[0]), S1 = pkh(s2[1]), N0 = pkh(-s2[0]), N1 = pkh(-s2[1]);
    A2re = u4{C0, C1, S0, S1}; A2im = u4{N0, N1, C0, C1};
    B3re = u4{C0, C1, N0, N1}; B3im = u4{S0, S1, C0, C1};
    const u32 D0 = pkh(c4[0]), D1 = pkh(c4[1]);
    A4re = u4{D0, D1, pkh(-s4[0]), pkh(-s4[1])};
    A4im = u4{pkh(s4[0]), pkh(s4[1]), D0, D1};
  }
  t3c *= 2.0f; t3s *= 2.0f;
  PIN(A2re); PIN(A2im); PIN(B3re); PIN(B3im); PIN(t3c); PIN(t3s); PIN(B1re); PIN(B1im); PIN(A4re); PIN(A4im);
  SB();
  PIN(w0); PIN(w1);
  f2 sr[2], si[2], dr[2], di[2];
#pragma unroll
  for (int p = 0; p < 2; p++) {
    const u4 w = p ? w1 : w0;
    const f2 a = unbf(w[0]), c = unbf(w[1]), e = unbf(w[2]), f = unbf(w[3]);
    sr[p] = a + e; si[p] = c + f; dr[p] = a - e; di[p] = c - f;
  }
  if (g == 0 || g == 320) {
    f2 w1r[2], w1i[2], w2r[2], w2i[2];
    u4 pmv[4];
#pragma unroll
    for (int r = 0; r < 4; r++) {
      const int kq = k1 + 4 * l15 + 64 * (4 * g4 + r), kk = (1024 - kq) & 1023;
      const int ee = kk >> 6;
      const char* pa = ws + WS_WT + ((size_t)(g * 4 + (kk & 3)) * 2 + ((ee >> 1) & 1)) * 1024 + (((kk >> 2) & 15) + 16 * (ee >> 2)) * 16;
      asm volatile("global_load_dwordx4 %0, %1, off" : "=v"(pmv[r]) : "v"(pa) : "memory");
    }
    asm volatile("s_waitcnt vmcnt(0)" : "+v"(pmv[0]), "+v"(pmv[1]), "+v"(pmv[2]), "+v"(pmv[3]) :: "memory");
#pragma unroll
    for (int r = 0; r < 4; r++) {
      const int kq = k1 + 4 * l15 + 64 * (4 * g4 + r), kk = (1024 - kq) & 1023;
      const int ee = kk >> 6;
      const u4 pm = pmv[r];
      const int sh = (ee & 1) * 16;
      const float m1r = bf2f((pm[0] >> sh) & 0xffffu), m1i = bf2f((pm[1] >> sh) & 0xffffu);
      const float m2r = bf2f((pm[2] >> sh) & 0xffffu), m2i = bf2f((pm[3] >> sh) & 0xffffu);
      const u4 w = r < 2 ? w0 : w1;
      const int so = (r & 1) * 16;
      const float a1r = bf2f((w[0] >> so) & 0xffffu), a1i = bf2f((w[1] >> so) & 0xffffu);
      const float a2r = bf2f((w[2] >> so) & 0xffffu), a2i = bf2f((w[3] >> so) & 0xffffu);
      const float p1r = kq == 0 ? m1r : m2r, p1i = kq == 0 ? m1i : m2i;
      const float p2r = kq == 0 ? m2r : m1r, p2i = kq == 0 ? m2i : m1i;
      w1r[r >> 1][r & 1] = 0.5f * (a1r + p1r); w1i[r >> 1][r & 1] = 0.5f * (a1i - p1i);
      w2r[r >> 1][r & 1] = 0.5f * (a2r + p2r); w2i[r >> 1][r & 1] = 0.5f * (a2i - p2i);
    }
#pragma unroll
    for (int p = 0; p < 2; p++) { sr[p] = w1r[p] + w2r[p]; si[p] = w1i[p] + w2i[p]; dr[p] = w1r[p] - w2r[p]; di[p] = w1i[p] - w2i[p]; }
  }
#pragma unroll
  for (int p = 0; p < 2; p++) { PIN(sr[p]); PIN(si[p]); PIN(dr[p]); PIN(di[p]); }
  SB();
#pragma unroll
  for (int i = 0; i < 4; i++) PIN(pv[i]);
  const f4 z4 = {0, 0, 0, 0};
  const u4 Pa = u4{pv[0][0], pv[1][0], pv[2][0], pv[3][0]};
  const u4 Pb = u4{pv[0][1], pv[1][1], pv[2][1], pv[3][1]} ^ 0x80000000u;
  const f4 pre0 = mfma_h(Pa, B1re, z4), pim0 = mfma_h(Pa, B1im, z4);
  const f4 pre1 = mfma_h(Pb, B1re, z4), pim1 = mfma_h(Pb, B1im, z4);
  const u4 Bf0 = cmul_pack_h(pre0, pim0, t1c, t1s), Bf1 = cmul_pack_h(pre1, pim1, t1c, t1s);
  const f4 zr = mfma_h(A2re, Bf0, z4), zi = mfma_h(A2im, Bf0, z4);
  const f4 yr = mfma_h(A2re, Bf1, z4), yi = mfma_h(A2im, Bf1, z4);
  u4 Aq0, Aq1;
#pragma unroll
  for (int p = 0; p < 2; p++) {
    const f2 zr2 = p ? hi2(zr) : lo2(zr), zi2 = p ? hi2(zi) : lo2(zi), yr2 = p ? hi2(yr) : lo2(yr), yi2 = p ? hi2(yi) : lo2(yi);
    const f2 c2 = p ? hi2(pc) : lo2(pc), s2 = p ? hi2(ps) : lo2(ps);
    const f2 ar = s2 * zr2 + c2 * yi2, ai = s2 * zi2 - c2 * yr2;
    const f2 br = s2 * yr2 + c2 * zi2, bi = s2 * yi2 - c2 * zr2;
    const f2 fr0 = sr[p] * zr2 - si[p] * zi2 - dr[p] * ar + di[p] * ai;
    const f2 fi0 = sr[p] * zi2 + si[p] * zr2 - dr[p] * ai - di[p] * ar;
    const f2 fr1 = sr[p] * yr2 - si[p] * yi2 + dr[p] * br - di[p] * bi;
    const f2 fi1 = sr[p] * yi2 + si[p] * yr2 + dr[p] * bi + di[p] * br;
    Aq0[p] = pkh(fr0); Aq0[2 + p] = pkh(fi0);
    Aq1[p] = pkh(fr1); Aq1[2 + p] = pkh(fi1);
  }
  const f4 qre0 = mfma_h(Aq0, B3re, z4), qim0 = mfma_h(Aq0, B3im, z4);
  const f4 qre1 = mfma_h(Aq1, B3re, z4), qim1 = mfma_h(Aq1, B3im, z4);
  const u4 Bg0 = cmul_pack_h(qre0, qim0, t3c, t3s), Bg1 = cmul_pack_h(qre1, qim1, t3c, t3s);
  const f4 y0r = mfma_h(A4re, Bg0, z4), y0i = mfma_h(A4im, Bg0, z4);
  const f4 y1r = mfma_h(A4re, Bg1, z4), y1i = mfma_h(A4im, Bg1, z4);
#pragma unroll
  for (int r = 0; r < 4; r++) *(u2*)(O + oaddr + r * 128) = u2{pkh(y0r[r], y0i[r]), pkh(y1r[r], -y1i[r])};
  __syncthreads();
  char* T2 = ws + WS_T2 + g * 32 + (t & 1) * 16 + (size_t)(t >> 1) * (ROWP * 4);
  const u4 v0 = *(const u4*)(O + 4 * t), v1 = *(const u4*)(O + 4 * t + 1024);
  *(u4*)(T2) = v0;
  *(u4*)(T2 + (size_t)128 * (ROWP * 4)) = v1;
}

constexpr int K3_RSZ = (9 * 321 + 16 * 6) * 4;
constexpr int K3_R = 0, K3_CB = (K3_RSZ + 15) & ~15, K3_Q = K3_CB + 18432, K3_MOD = K3_Q + 4096, K3_EX = K3_MOD + 8192,
              K3_SH = K3_EX + 12288, K3_SX = K3_SH + 10240, K3_LDS = K3_SX + 10240;
DI void k3_body(const u16* __restrict__ x, char* __restrict__ ws, char* lds,
               __amdgpu_buffer_rsrc_t rw1, int wvo, u4 (&w0)[4], u4 (&w1)[4]) {
  const u32* Rw = (const u32*)(lds + K3_R);
  char* CB = lds + K3_CB;
  char* EX = lds + K3_EX;
  char* SH = lds + K3_SH;
  char* SX = lds + K3_SX;
  const int s2 = xcd_map(blockIdx.x, 32);
  const int t = threadIdx.x, l = t & 63, l15 = l & 15, g4 = l >> 4;
  const int wv = __builtin_amdgcn_readfirstlane(t >> 6);
  const char* T2 = ws + WS_T2 + (size_t)s2 * (ROWP * 4);
  const u4 r0 = *(const u4*)(T2 + t * 16);
  const u4 r1 = *(const u4*)(T2 + (t < 130 ? t + 512 : t) * 16);
  u4 tb[4];
  {
    const char* p2 = wv < 2 ? (const char*)CB2_TAB + (16 + wv) * 1024
                            : (wv < 6 ? (const char*)RQ_TAB + (4 + wv - 2) * 1024 : ws + WS_MOD + 2 * 2560 + (wv - 6) * 1024);
    const char* p3 = ws + WS_MOD + 2 * 2560 + (wv < 6 ? wv + 2 : 2) * 1024;
    tb[0] = gload16((const char*)CB2_TAB + wv * 1024 + l * 16);
    tb[1] = gload16((const char*)CB2_TAB + (8 + wv) * 1024 + l * 16);
    tb[2] = gload16(p2 + l * 16);
    tb[3] = gload16(p3 + l * 16);
  }
  const int s1p = wv >> 1, par = wv & 1;
  const int row = 2 * s2 + 512 * s1p + par;
  u2 xv[3];
  const int d0 = 40 * l15 + 4 * g4;
  float unsc = *(const float*)(ws + WS_SCL);
  SB();
  const int k1 = wv & 3, ct0 = wv < 4 ? 0 : 2;
  f4 twc[2], tws[2];
  {
    const float c0 = (float)(l15 + (wv < 4 ? 0 : 24)) * (1.0f / 640.0f), c1 = c0 + 16.0f / 640.0f;
    const float fg = (float)(4 * g4);
    const f2 s0 = f2{vcos(c0), vsin(c0)}, s1 = f2{vcos(c1), vsin(c1)};
    const f2 a0 = f2{vcos(c0 * fg), vsin(c0 * fg)}, a1 = f2{vcos(c1 * fg), vsin(c1 * fg)};
    const f2 a01 = cmul(a0, s0), a02 = cmul(a01, s0), a03 = cmul(a02, s0);
    const f2 a11 = cmul(a1, s1), a12 = cmul(a11, s1), a13 = cmul(a12, s1);
    twc[0] = f4{a0[0], a01[0], a02[0], a03[0]}; tws[0] = f4{a0[1], a01[1], a02[1], a03[1]};
    twc[1] = f4{a1[0], a11[0], a12[0], a13[0]}; tws[1] = f4{a1[1], a11[1], a12[1], a13[1]};
  }
  const int alt = 2 * ((4 - k1) & 3) + 1;
  int G[12];
#pragma unroll
  for (int z = 0; z < 12; z++) {
    int j2 = 16 * (z >> 2) + 4 * g4 + (z & 3);
    if (z >= 8) j2 = j2 > 39 ? 39 : j2;
    const int kd = l15 + 16 * j2;
    const bool first = (z < 4) || (z < 8 && kd <= 320);
    const int g = first ? kd : 640 - kd;
    G[z] = 9 * g + 16 * (g >> 6) + (first ? 2 * k1 : alt);
    PIN(G[z]);
  }
  PIN(twc[0]); PIN(tws[0]); PIN(twc[1]); PIN(tws[1]);
  SB();
  {
    u32* Rd = (u32*)(lds + K3_R);
    const int g0 = t >> 1, a0 = 9 * g0 + 16 * (g0 >> 6) + 4 * (t & 1);
#pragma unroll
    for (int e = 0; e < 4; e++) Rd[a0 + e] = r0[e];
    if (t < 130) {
      const int g1 = (t + 512) >> 1, a1 = 9 * g1 + 16 * (g1 >> 6) + 4 * (t & 1);
#pragma unroll
      for (int e = 0; e < 4; e++) Rd[a1 + e] = r1[e];
    }
  }
  *(u4*)(CB + wv * 1024 + l * 16) = tb[0];
  *(u4*)(CB + (8 + wv) * 1024 + l * 16) = tb[1];
  *(u4*)(CB + (16 + wv) * 1024 + l * 16) = tb[2];
  if (wv < 6) *(u4*)(CB + (24 + wv) * 1024 + l * 16) = tb[3];
  PIN(unsc);
  __syncthreads();
#pragma unroll
  for (int mt = 0; mt < 3; mt++) xv[mt] = *(const u2*)(x + (size_t)row * D_ + d0 + offt(mt));
  SB();
#pragma unroll
  for (int z = 0; z < 4; z++) w0[z] = bload(rw1, wvo, (8 * z) * (F_ * 2));
#pragma unroll
  for (int z = 0; z < 4; z++) w1[z] = bload(rw1, wvo, (128 + 8 * z) * (F_ * 2));
  SB();
  u4 Af[3];
  u4 cf[6], cg[6];
  {
#pragma unroll
    for (int i = 0; i < 4; i++) { Af[0][i] = Rw[G[i]]; Af[1][i] = Rw[G[4 + i]]; Af[2][i] = Rw[G[8 + i]]; }
    const char* cb = CB + ct0 * 6 * 1024 + l * 16;
#pragma unroll
    for (int y = 0; y < 6; y++) cf[y] = *(const u4*)(cb + y * 1024);
    if (wv < 4) {
#pragma unroll
      for (int y = 0; y < 6; y++) cg[y] = *(const u4*)(cb + (6 + y) * 1024);
    }
  }
  u4 Qb[4];
#pragma unroll
  for (int kk = 0; kk < 4; kk++) Qb[kk] = *(const u4*)(lds + K3_Q + ((s1p * kk - par) & 3) * 1024 + l * 16);
  f4 gav[3], sbv[3], bv[3];
#pragma unroll
  for (int mt = 0; mt < 3; mt++) {
    const char* md = lds + K3_MOD + 4 * (d0 + offt(mt));
    gav[mt] = *(const f4*)(md) * unsc;
    sbv[mt] = *(const f4*)(md + 2560);
    bv[mt] = *(const f4*)(md + 5120);
  }
  SB();
  {
    f4 ere = {0, 0, 0, 0}, eim = {0, 0, 0, 0};
#pragma unroll
    for (int kp = 0; kp < 3; kp++) {
      ere = mfma_h(Af[kp], cf[2 * kp], ere);
      eim = mfma_h(Af[kp], cf[2 * kp + 1], eim);
    }
    if (wv < 4) {
      f4 fre = {0, 0, 0, 0}, fim = {0, 0, 0, 0};
#pragma unroll
      for (int kp = 0; kp < 3; kp++) {
        fre = mfma_h(Af[kp], cg[2 * kp], fre);
        fim = mfma_h(Af[kp], cg[2 * kp + 1], fim);
      }
      *(u4*)(EX + (k1 * 3 + 1) * 1024 + l * 16) = cmul_pack_h(fre, fim, twc[1], tws[1]);
    }
    *(u4*)(EX + (k1 * 3 + ct0) * 1024 + l * 16) = cmul_pack_h(ere, eim, twc[0], tws[0]);
  }
  __syncthreads();
  {
    u4 ef[12];
#pragma unroll
    for (int z = 0; z < 12; z++) ef[z] = *(const u4*)(EX + z * 1024 + l * 16);
    SB();
    f4 y[3];
#pragma unroll
    for (int mt = 0; mt < 3; mt++) y[mt] = f4{0, 0, 0, 0};
#pragma unroll
    for (int kk = 0; kk < 4; kk++)
#pragma unroll
      for (int mt = 0; mt < 3; mt++) y[mt] = mfma_h(ef[kk * 3 + mt], Qb[kk], y[mt]);
    f2 x1[3][2];
    u32 x1p[3][2];
    const float vmask = g4 >= 2 ? 1.f : 0.f;
    const float x0 = __builtin_bit_cast(float, __builtin_amdgcn_readfirstlane(xv[0][0] << 16));
    f2 sum2 = {0.f, 0.f}, sq2 = {0.f, 0.f};
#pragma unroll
    for (int mt = 0; mt < 3; mt++) {
#pragma unroll
      for (int hh = 0; hh < 2; hh++) {
        const f2 yy = rbf2(hh ? hi2(y[mt]) : lo2(y[mt]));
        const f2 gy = rbf2((hh ? hi2(gav[mt]) : lo2(gav[mt])) * yy);
        x1p[mt][hh] = pkbf(unbf(xv[mt][hh]) + gy);
        x1[mt][hh] = unbf(x1p[mt][hh]) - x0;
        const f2 dl = (mt == 2) ? x1[mt][hh] * vmask : x1[mt][hh];
        sum2 += dl;
        sq2 += dl * dl;
      }
    }
    float sum = sum2[0] + sum2[1], sq = sq2[0] + sq2[1];
    wave_sum2(sum, sq);
    const float mean = sum * (1.0f / 640.0f);
    const float var = fmaxf(sq * (1.0f / 640.0f) - mean * mean, 0.f);
    const float rstd = __builtin_amdgcn_rsqf(var + 1e-5f);
    char* sh = SH + wv * 1280 + 2 * d0;
#pragma unroll
    for (int mt = 0; mt < 3; mt++) {
      if (mt < 2 || g4 >= 2) {
        const f2 h0 = (x1[mt][0] - mean) * (lo2(bv[mt]) * rstd) + lo2(sbv[mt]);
        const f2 h1 = (x1[mt][1] - mean) * (hi2(bv[mt]) * rstd) + hi2(sbv[mt]);
        *(u2*)(sh + 2 * offt(mt)) = u2{pkbf(h0), pkbf(h1)};
        *(u2*)(sh + 10240 + 2 * offt(mt)) = u2{x1p[mt][0], x1p[mt][1]};
      }
    }
  }
  __syncthreads();
  const int tile = s2 >> 1;
  const __amdgpu_buffer_rsrc_t rh2 = __builtin_amdgcn_make_buffer_rsrc(
      ws + WS_H2 + (size_t)tile * HST + 8 * (s2 & 1) * 16, 0, HST, 0x00020000);
  const __amdgpu_buffer_rsrc_t rx1 = __builtin_amdgcn_make_buffer_rsrc(
      ws + WS_X1 + (size_t)(16 * tile + 8 * (s2 & 1)) * 1280, 0, 8 * 1280, 0x00020000);
  {
    const int rr = t & 7, gam = t >> 3;
    const u4 v = *(const u4*)(SH + rr * 1280 + gam * 16);
    const u4 w = *(const u4*)(SX + t * 16);
    u4 v2 = v, w2 = w;
    if (t < 128) { v2 = *(const u4*)(SH + rr * 1280 + (gam + 64) * 16); w2 = *(const u4*)(SX + (t + 512) * 16); }
    __builtin_amdgcn_raw_buffer_store_b128(v, rh2, gam * 256 + rr * 16, 0, 16);
    __builtin_amdgcn_raw_buffer_store_b128(v2, rh2, t < 128 ? (gam + 64) * 256 + rr * 16 : 0x7ff00000, 0, 16);
    SB();
    __builtin_amdgcn_raw_buffer_store_b128(w, rx1, t * 16, 0, 16);
    __builtin_amdgcn_raw_buffer_store_b128(w2, rx1, t < 128 ? (t + 512) * 16 : 0x7ff00000, 0, 16);
  }
}

__host__ __device__ constexpr int wimg(int k) { return 320 * k; }
constexpr int K4_SLOT = wimg(128), K45_LDS = 4 * K4_SLOT;
typedef __attribute__((address_space(3))) void* ldsv;
typedef __attribute__((address_space(1))) u32 gu32;
#define WAITV(n) asm volatile("s_waitcnt vmcnt(" #n ")" ::: "memory")
#define WAITL(n) asm volatile("s_waitcnt lgkmcnt(" #n ")" ::: "memory")
#define BAR() __builtin_amdgcn_s_barrier()
DI u4 bload_sc1(__amdgpu_buffer_rsrc_t r, int voff, int soff) {
  return __builtin_bit_cast(u4, __builtin_amdgcn_raw_buffer_load_b128(r, voff, soff, 16));
}
constexpr int POLL_SLEEP = 4;
DI void poll_ge(const gu32* p, u32 target) {
  u32 a, b;
  asm volatile("global_load_dword %0, %1, off sc1" : "=v"(a) : "v"(p) : "memory");
  for (int spin = 0; spin < (1 << 16); spin++) {
    __builtin_amdgcn_s_sleep(POLL_SLEEP);
    asm volatile("global_load_dword %0, %2, off sc1\n\ts_waitcnt vmcnt(1)" : "=&v"(b), "+v"(a) : "v"(p) : "memory");
    if (__builtin_amdgcn_readfirstlane(a) >= target) { asm volatile("s_waitcnt vmcnt(0)" : "+v"(b) :: "memory"); return; }
    __builtin_amdgcn_s_sleep(POLL_SLEEP);
    asm volatile("global_load_dword %0, %2, off sc1\n\ts_waitcnt vmcnt(1)" : "=&v"(a), "+v"(b) : "v"(p) : "memory");
    if (__builtin_amdgcn_readfirstlane(b) >= target) { asm volatile("s_waitcnt vmcnt(0)" : "+v"(a) :: "memory"); return; }
  }
  asm volatile("s_waitcnt vmcnt(0)" : "+v"(a), "+v"(b) :: "memory");
}
constexpr int NEARLY = 6, NLDS = 8;
DI constexpr int wd_lds(int ks, int s0_) {
  const int s = s0_ >= NLDS ? s0_ - NLDS : s0_;
  const int piece = s < NEARLY ? (ks * NEARLY + s) * 5 : 20 * NEARLY + (ks * (NLDS - NEARLY) + s - NEARLY) * 5;
  return (K4_SLOT + piece * 1024) % K45_LDS;
}
DI void glds_asm(__amdgpu_buffer_rsrc_t r, u32 lds_dst, int voff, int soff) {
  asm volatile("s_mov_b32 m0, %2\n\ts_nop 0\n\tbuffer_load_dwordx4 %0, %1, %3 offen lds" :: "v"(voff), "s"(r), "s"(lds_dst), "s"(soff) : "memory");
}

static_assert(K3_LDS <= 2 * K4_SLOT, "K3 scratch must fit ring slots 2,3");
__global__ __launch_bounds__(512, 2) void k345(const u16* __restrict__ xin, const u16* __restrict__ wg, const u16* __restrict__ wu,
                                               char* __restrict__ ws, u16* __restrict__ out) {
  extern __shared__ __attribute__((aligned(16))) char lds[];
  const int b = blockIdx.x;
  const int x = b & 7, r = b >> 3;
  const int t = threadIdx.x, l = t & 63, l15 = l & 15, g4 = l >> 4;
  const int wv = __builtin_amdgcn_readfirstlane(t >> 6);
  gu32* cnt = (gu32*)(uintptr_t)(ws + WS_CNT);
  const int tb2 = 8 * (x & 3) + (r & 7), cc = 4 * (x >> 2) + (r >> 3);
  const int ks = wv & 3;
  const int s0 = ks < 3 ? 14 * ks : 41, ns = ks < 2 ? 14 : 13;
  const __amdgpu_buffer_rsrc_t rw2 = __builtin_amdgcn_make_buffer_rsrc(
      ws + WS_WD + (size_t)cc * 54 * 5 * 1024 + (size_t)s0 * 5 * 1024, 0, ns * 5 * 1024, 0x00020000);
  const u32 ldsb = (u32)(size_t)lds;
  const int wh = wv >> 2;
  bool early = false;
  constexpr int EPW = NEARLY * 5 / 2;
  const u32 dz = ldsb + K4_SLOT + (ks * NEARLY * 5 + wh * EPW) * 1024;
  const int sz = wh * EPW * 1024;
  const int tb1 = 4 * (x & 1) + (r & 3), j1 = 7 * (x >> 1) + (r >> 2);
  const bool has1 = r < 28 && j1 < 27;
  const int arr = wv >> 2, wq = wv & 3;
  const __amdgpu_buffer_rsrc_t rw1 = __builtin_amdgcn_make_buffer_rsrc((void*)(arr ? wu : wg), 0, D_ * F_ * 2, 0x00020000);
  const int wvo = has1 ? (32 * wq + (l >> 3)) * (F_ * 2) + 128 * j1 + 16 * (l & 7) : 0x7ff00000;
  char* wld = lds + wimg(32 * wq + (l >> 3)) + ((l & 7) >> 1) * 64 + arr * 32 + (l & 1) * 16;
  u4 wst[5][4];
#define WLOAD1(p, z) wst[p][z] = bload(rw1, wvo, ((p) * 128 + 8 * (z)) * (F_ * 2))
#define WSTORE1(p, z) *(u4*)(wld + ((p) & 3) * K4_SLOT + wimg(8 * (z))) = wst[p][z]
  k3_body(xin, ws, lds + 2 * K4_SLOT, rw1, wvo, wst[0], wst[1]);
  WAITV(2);
  __syncthreads();
  if (t == 0) __hip_atomic_fetch_add(cnt + 64 * (16 + x), 1u, __ATOMIC_RELAXED, __HIP_MEMORY_SCOPE_AGENT);
#pragma unroll
  for (int z = 0; z < 4; z++) { WSTORE1(0, z); WSTORE1(1, z); }
  WAITV(0);
  __syncthreads();
  if (t == 0) __hip_atomic_fetch_add(cnt + 64 * (24 + x), 1u, __ATOMIC_RELAXED, __HIP_MEMORY_SCOPE_AGENT);
  {
    const int tb = tb1, j = j1;
    if (has1) {
      if (wv == 0) poll_ge(cnt + 64 * (16 + tb), 32u);
      WAITL(0);
      BAR();
      asm volatile("" ::: "memory");
      const __amdgpu_buffer_rsrc_t rh = __builtin_amdgcn_make_buffer_rsrc(
          ws + WS_H2 + (size_t)(16 * tb + 2 * wv) * HST, 0, 2 * HST, 0x00020000);
      const int l31 = l & 31, hh = l >> 5;
      const int tvo = (l31 >> 4) * HST + hh * 256 + (l31 & 15) * 16;
      const char* ldr = lds + wimg(8 * (g4 >> 1) + (l15 >> 2)) + (g4 & 1) * 32 + 8 * (l15 & 3);
      f16v acc[4];
#pragma unroll
      for (int i = 0; i < 4; i++)
#pragma unroll
        for (int e = 0; e < 16; e++) acc[i][e] = 0.f;
      constexpr int PF = 6;
      u4 tk[20][2];
#define TLD(k, hf) tk[k][hf] = bload_sc1(rh, tvo, (k) * 1024 + (hf) * 512)
#define AFRAG(kap, hf, i) cat(trread(ldr + (((kap) >> 2) & 3) * K4_SLOT + wimg(32 * ((kap) & 3) + 16 * (hf)) + (i) * 64), \
                              trread(ldr + (((kap) >> 2) & 3) * K4_SLOT + wimg(32 * ((kap) & 3) + 16 * (hf) + 4) + (i) * 64))
#pragma unroll
      for (int k = 0; k < PF; k++)
#pragma unroll
        for (int hf = 0; hf < 2; hf++) { TLD(k, hf); SB(); }
      u4 af[2][8];
#pragma unroll
      for (int i = 0; i < 8; i++) af[0][i] = AFRAG(0, i >> 2, i & 3);
#pragma unroll
      for (int kap = 0; kap < 20; kap++) {
        const int p = kap >> 2, kk = kap & 3;
        SB();
        if (kk == 3 && p + 1 < 5) __syncthreads();
        if (kap == 16) {
#pragma unroll
          for (int k = 16; k < 20; k++) { PIN(tk[k][0]); PIN(tk[k][1]); }
          __syncthreads();
        }
#pragma unroll
        for (int i = 0; i < 8; i++) {
          if (kap >= 16 && kap < 20 && (i & 1) == 0 && (kap - 16) * 4 + (i >> 1) < EPW) {
            const int z0 = (kap - 16) * 4 + (i >> 1);
            glds_asm(rw2, dz + z0 * 1024, l * 16, sz + z0 * 1024);
          }
          if (kap + 1 < 20) af[(kap + 1) & 1][i] = AFRAG(kap + 1, i >> 2, i & 3);
          if (i < 2 && kap + PF < 20) TLD(kap + PF, i);
          if (kk == 2 && p >= 1 && p + 1 < 5 && i >= 4) WSTORE1(p + 1, i - 4);
          if (kk == 0 && p + 2 < 5 && i >= 4) WLOAD1(p + 2, i - 4);
          SB();
          acc[i & 3] = __builtin_amdgcn_mfma_f32_32x32x16_bf16(__builtin_bit_cast(b8, af[kap & 1][i]), __builtin_bit_cast(b8, tk[kap][i >> 2]),
                                                               acc[i & 3], 0, 0, 0);
          SB();
        }
      }
      const __amdgpu_buffer_rsrc_t rt = __builtin_amdgcn_make_buffer_rsrc(
          ws + WS_TT + (size_t)(16 * tb + 2 * wv) * TST + (size_t)(8 * j) * 256, 0, TST + 8 * 256, 0x00020000);
      const int so = (l31 >> 4) * TST + hh * 256 + (l31 & 15) * 16;
#pragma unroll
      for (int i = 0; i < 4; i++) {
        u32 X[4];
#pragma unroll
        for (int e = 0; e < 4; e++) {
          const u32 g = pkbf(acc[i][2 * e], acc[i][2 * e + 1]), u = pkbf(acc[i][8 + 2 * e], acc[i][8 + 2 * e + 1]);
          X[e] = pkbf(unbf(g) * unbf(u));
        }
        auto a = __builtin_amdgcn_permlane32_swap(X[0], X[2], false, false);
        auto c = __builtin_amdgcn_permlane32_swap(X[1], X[3], false, false);
        const u4 v = u4{a[0], c[0], a[1], c[1]};
        __builtin_amdgcn_raw_buffer_store_b128(v, rt, so, i * 512, 16);
        SB();
      }
      WAITV(0);
      BAR();
      if (t == 0) __hip_atomic_fetch_add(cnt + 64 * tb, 1u, __ATOMIC_RELAXED, __HIP_MEMORY_SCOPE_AGENT);
      early = true;
    }
  }
  const int tb = tb2;
  const __amdgpu_buffer_rsrc_t rw = rw2;
  const int vo = l * 16;
  if (wv >= 4) {
    if (!early) {
#pragma unroll
      for (int z = 0; z < NEARLY * 5; z++) glds_asm(rw, ldsb + wd_lds(ks, z / 5) + (z % 5) * 1024, vo, z * 1024);
    }
#pragma unroll
    for (int z = NEARLY * 5; z < NLDS * 5; z++) glds_asm(rw, ldsb + wd_lds(ks, z / 5) + (z % 5) * 1024, vo, z * 1024);
    WAITV(0);
    BAR();
#define WRING(s) { _Pragma("unroll") for (int i_ = 0; i_ < 5; i_++) \
      glds_asm(rw, ldsb + wd_lds(ks, s) + i_ * 1024, vo, ((s) * 5 + i_) * 1024); }
    BAR();  WRING(8); WRING(9);
    BAR();  WRING(10); WRING(11);
    WAITV(10); BAR(); WRING(12); WRING(13);
    WAITV(10); BAR();
    WAITV(0); BAR();
    return;
  }
  const __amdgpu_buffer_rsrc_t rt = __builtin_amdgcn_make_buffer_rsrc(
      ws + WS_TT + (size_t)(4 * tb) * TST + s0 * 1024, 0, 3 * TST + ns * 1024, 0x00020000);
  const int last_t = ns == 14 ? 13 * 1024 : 0x7ff00000 ;
  f4 acc[4][5];
#pragma unroll
  for (int m = 0; m < 4; m++)
#pragma unroll
    for (int i = 0; i < 5; i++) acc[m][i] = f4{0, 0, 0, 0};
  u4 tk[14][4], wf[2][5];
  constexpr int PF = 5;
#define TLOAD(s, m) tk[s][m] = bload_sc1(rt, vo, ((s) == 13 ? last_t : (s) * 1024) + ((s) == 13 && ns != 14 ? 0 : (m) * TST))
  const int c15 = l & 15, q4 = l >> 4;
  if (wv == 0) {
    poll_ge(cnt + 64 * (24 + (tb >> 2)), 32u);
    poll_ge(cnt + 64 * (tb >> 2), 27u);
  }
  WAITV(0);
  BAR();
  asm volatile("" ::: "memory");
#pragma unroll
  for (int s = 0; s < PF; s++) {
#pragma unroll
    for (int m = 0; m < 4; m++) TLOAD(s, m);
    SB();
  }
  const char* wl = lds + vo;
#pragma unroll
  for (int i = 0; i < 5; i++) wf[0][i] = *(const u4*)(wl + wd_lds(ks, 0) + i * 1024);
  f4 gbv[5];
  u2 xw[5];
  const __amdgpu_buffer_rsrc_t rx1 = __builtin_amdgcn_make_buffer_rsrc(
      ws + WS_X1 + (size_t)(16 * (4 * tb + ks)) * 1280, 0, 16 * 1280, 0x00020000);
#pragma unroll
  for (int s = 0; s < 14; s++) {
    if (s == 2 || s == 4 || s == 6 || s == 8 || s == 10) { SB(); WAITL(0); BAR(); asm volatile("" ::: "memory"); }
#pragma unroll
    for (int i = 0; i < 5; i++) {
      SB();
      if (s + PF < 14 && i < 4) TLOAD(s + PF, i);
      if (s + 1 < 14) wf[(s + 1) & 1][i] = *(const u4*)(wl + wd_lds(ks, s + 1) + i * 1024);
      if (s == 9) {
        const int d = 80 * cc + 16 * i + 4 * q4;
        gbv[i] = *(const f4*)((const float*)(ws + WS_MOD) + 5 * 640 + d);
        xw[i] = __builtin_bit_cast(u2, __builtin_amdgcn_raw_buffer_load_b64(rx1, c15 * 1280 + 2 * d, 0, 16));
      }
      SB();
#pragma unroll
      for (int m = 0; m < 4; m++) acc[m][i] = mfma_b(wf[s & 1][i], tk[s][m], acc[m][i]);
    }
  }
  SB();
  BAR();
#pragma unroll
  for (int m = 0; m < 4; m++) {
    if (m != ks) {
      const int slot = ks < m ? ks : ks - 1;
#pragma unroll
      for (int i = 0; i < 5; i++) *(f4*)(lds + ((m * 3 + slot) * 5 + i) * 1024 + l * 16) = acc[m][i];
    }
  }
  __syncthreads();
  {
    f4 own[5];
    if (ks == 0) { _Pragma("unroll") for (int i = 0; i < 5; i++) own[i] = acc[0][i]; }
    else if (ks == 1) { _Pragma("unroll") for (int i = 0; i < 5; i++) own[i] = acc[1][i]; }
    else if (ks == 2) { _Pragma("unroll") for (int i = 0; i < 5; i++) own[i] = acc[2][i]; }
    else { _Pragma("unroll") for (int i = 0; i < 5; i++) own[i] = acc[3][i]; }
    const char* sl = lds + ks * 15 * 1024 + l * 16;
    u16* orow = out + (size_t)tok_row(4 * tb + ks, c15) * D_ + 80 * cc + 4 * q4;
#pragma unroll
    for (int i = 0; i < 5; i++) {
      const f4 o = (own[i] + *(const f4*)(sl + i * 1024)) + (*(const f4*)(sl + (5 + i) * 1024) + *(const f4*)(sl + (10 + i) * 1024));
      const f2 r0 = unbf(xw[i][0]) + rbf2(lo2(gbv[i]) * rbf2(lo2(o)));
      const f2 r1 = unbf(xw[i][1]) + rbf2(hi2(gbv[i]) * rbf2(hi2(o)));
      *(u2*)(orow + 16 * i) = u2{pkbf(r0), pkbf(r1)};
    }
  }
}

extern "C" void kernel_launch(void* const* d_in, const int* in_sizes, int n_in, void* d_out, int out_size, void* d_ws,
                              size_t ws_size, hipStream_t stream) {
  const u16* x = (const u16*)d_in[0];
  const u16* wr = (const u16*)d_in[1];
  const u16* wi = (const u16*)d_in[2];
  const u16* wg = (const u16*)d_in[4];
  const u16* wu = (const u16*)d_in[5];
  const u16* wd = (const u16*)d_in[6];
  const u16* c = (const u16*)d_in[7];
  const u16* wmod = (const u16*)d_in[8];
  char* ws = (char*)d_ws;
  (void)hipFuncSetAttribute((const void*)k345, hipFuncAttributeMaxDynamicSharedMemorySize, K45_LDS);
  k0_mod<<<K0_GEMV, 256, 0, stream>>>(c, wmod, ws);
  k1_fwd<<<256, 512, 0, stream>>>(x, wr, wi, ws);
  k2_filter<<<K2_GRID, 256, 0, stream>>>(wd, ws);
  k345<<<256, 512, K45_LDS, stream>>>(x, wg, wu, ws, (u16*)d_out);
}
```
